```python
import torch

def _softmax(x, axis=-1):
    return torch.softmax(x, dim=axis)
def _gelu(x):
    return torch.nn.functional.gelu(x, approximate="tanh")
def _silu(x):
    return torch.nn.functional.silu(x)
def _sigmoid(x):
    return torch.sigmoid(x)
def _layernorm(x, axis=-1, eps=1e-5):
    m = x.mean(dim=axis, keepdim=True)
    v = x.var(dim=axis, keepdim=True, unbiased=False)
    return (x - m) / torch.sqrt(v + eps)
def _scatter_add(tgt, idx, src, axis=0):
    return tgt.clone().index_add_(axis, idx, src)
def _segment_sum(src, seg, n):
    out = torch.zeros((n,) + src.shape[1:], dtype=src.dtype)
    return out.index_add_(0, seg, src)
def _pad(x, pw):
    flat = tuple(v for pair in reversed(pw) for v in pair)
    return torch.nn.functional.pad(x, flat)

kernel_name = 'sequential_preLN__gqa_block_cached__mixer_token_mlp__decode'

def setup_inputs():

    _w = (lambda a: (lambda t: t.to(torch.bfloat16) if t.is_floating_point() else t)(torch.as_tensor(a)))
    def _A_setup():
        B, Sq, D = 8, 1, 448
        Hq, Hkv, Sk, Dh = 16, 8, 8192, 256
        assert Hq % Hkv == 0, (Hq, Hkv)
        return {
            "x": _w((lambda *s: torch.randn(*s, dtype=torch.bfloat16) * 0.05)(B, Sq, D)),
            "k_cache": _w((lambda *s: torch.randn(*s, dtype=torch.bfloat16) * 1.6)(B, Hkv, Sk, Dh)),
            "v_cache": _w((lambda *s: torch.randn(*s, dtype=torch.bfloat16) * 0.8)(B, Hkv, Sk, Dh)),
            "wq": _w((lambda *s: torch.randn(*s, dtype=torch.bfloat16) * 1.6)(D, Hq * Dh) * (D ** -0.5)),
            "wo": _w((lambda *s: torch.randn(*s, dtype=torch.bfloat16) * 0.8)(Hq * Dh, D) * ((Hq * Dh) ** -0.5)),
        }
    _A_in = _A_setup()

    def _B_setup():
        B, S, D, F = 8, 1, 448, 1792
        return {
            "x": _w((lambda *s: torch.randn(*s, dtype=torch.bfloat16) * 0.05)(B, S, D)),
            "w1": _w((lambda *s: torch.randn(*s, dtype=torch.bfloat16) * 0.4)(S, F) * (S ** -0.5)),
            "b1": _w((lambda *s: torch.randn(*s, dtype=torch.bfloat16) * 0.4)(F) * 0.02),
            "w2": _w((lambda *s: torch.randn(*s, dtype=torch.bfloat16) * 0.4)(F, S) * (F ** -0.5)),
            "b2": _w((lambda *s: torch.randn(*s, dtype=torch.bfloat16) * 0.4)(S) * 0.02),
        }
    _B_in = _B_setup()

    return {
        "A_x": _A_in["x"],
        "A_k_cache": _A_in["k_cache"],
        "A_v_cache": _A_in["v_cache"],
        "A_wq": _A_in["wq"],
        "A_wo": _A_in["wo"],
        "B_x": _B_in["x"],
        "B_w1": _B_in["w1"],
        "B_b1": _B_in["b1"],
        "B_w2": _B_in["w2"],
        "B_b2": _B_in["b2"],
    }

def reference(A_x, A_k_cache, A_v_cache, A_wq, A_wo, B_x, B_w1, B_b1, B_w2, B_b2):
    def _A_ref(x, k_cache, v_cache, wq, wo):
        B, Sq, D = x.shape
        Hq, Dh = 16, 256
        Hkv = k_cache.shape[1]
        rep = Hq // Hkv
        q = (lambda x, *d: x.permute(*d))((x @ wq).reshape(B, Sq, Hq, Dh), 0, 2, 1, 3)
        k = (lambda x, n, axis: torch.repeat_interleave(x, n, dim=axis))(k_cache, rep, axis=1)
        v = (lambda x, n, axis: torch.repeat_interleave(x, n, dim=axis))(v_cache, rep, axis=1)
        scores = torch.einsum("bhqd,bhkd->bhqk", q, k) * (Dh ** -0.5)
        p = _softmax(scores, axis=-1)
        o = torch.einsum("bhqk,bhkd->bhqd", p, v)
        return (lambda x, *d: x.permute(*d))(o, 0, 2, 1, 3).reshape(B, Sq, Hq * Dh) @ wo

    def _B_ref(x, w1, b1, w2, b2):
        B, S, D = x.shape
        h = _layernorm(x, axis=-1)
        u = torch.einsum("bsd,sf->bdf", h, w1) + b1
        u = _gelu(u)
        y = torch.einsum("bdf,fs->bds", u, w2) + b2
        return (lambda x, *d: x.permute(*d))(y, 0, 2, 1)

    x = A_x
    h = x + _A_ref(_layernorm(x, axis=-1), A_k_cache, A_v_cache, A_wq, A_wo)
    h2 = h + _B_ref(_layernorm(h, axis=-1), B_w1, B_b1, B_w2, B_b2)
    return h2
```

```cpp
#include <hip/hip_runtime.h>
#include <hip/hip_bf16.h>
#include <cstdint>

namespace {
constexpr int B = 8, D = 448, HQ = 16, HKV = 8, SK = 8192, DH = 256, F = 1792;
constexpr int NSPLIT = 4, KEYS_PER_SPLIT = SK / NSPLIT;
constexpr int NW = 8, NTHR = NW * 64;
constexpr int TILE = 16;
constexpr int TILES = KEYS_PER_SPLIT / TILE;
constexpr float EPS = 1e-5f;

typedef unsigned u32x4 __attribute__((ext_vector_type(4)));
typedef float f32x4 __attribute__((ext_vector_type(4)));
typedef __bf16 bf16x2 __attribute__((ext_vector_type(2)));

__device__ __forceinline__ float bf_lo(unsigned u) { return __builtin_bit_cast(float, u << 16); }
__device__ __forceinline__ float bf_hi(unsigned u) { return __builtin_bit_cast(float, u & 0xffff0000u); }
__device__ __forceinline__ float bf1(unsigned short u) { return __builtin_bit_cast(float, (unsigned)u << 16); }
__device__ __forceinline__ float dot2(unsigned a, unsigned b, float acc) {
  return __builtin_amdgcn_fdot2_f32_bf16(__builtin_bit_cast(bf16x2, a), __builtin_bit_cast(bf16x2, b), acc, false);
}
__device__ __forceinline__ unsigned cvtpk(float lo, float hi) {
  unsigned r; asm volatile("v_cvt_pk_bf16_f32 %0, %1, %2" : "=v"(r) : "v"(lo), "v"(hi)); return r;
}
template <int CTRL> __device__ __forceinline__ float dpp(float x) {
  return __builtin_bit_cast(float, __builtin_amdgcn_mov_dpp(__builtin_bit_cast(int, x), CTRL, 0xf, 0xf, true));
}
constexpr int XOR1 = 0xB1, XOR2 = 0x4E, XOR7 = 0x141, ROR8 = 0x128, ROR4 = 0x124;
__device__ __forceinline__ float wave_sum(float x) {
  x += dpp<XOR1>(x); x += dpp<XOR2>(x); x += dpp<XOR7>(x); x += dpp<ROR8>(x);
  auto s = __builtin_amdgcn_permlane16_swap(__float_as_uint(x), __float_as_uint(x), false, false);
  x = __uint_as_float(s[0]) + __uint_as_float(s[1]);
  auto t = __builtin_amdgcn_permlane32_swap(__float_as_uint(x), __float_as_uint(x), false, false);
  return __uint_as_float(t[0]) + __uint_as_float(t[1]);
}

constexpr size_t WS_Q = 0;
constexpr size_t WS_ML = 65536;
constexpr size_t WS_PO = 131072;
constexpr size_t WS_POUT = WS_PO + (size_t)B * HQ * NSPLIT * DH * 4;

__global__ __launch_bounds__(256) void k_qproj(const unsigned short* __restrict__ x, const unsigned short* __restrict__ wq,
                                               unsigned short* __restrict__ q) {
  __shared__ float xn[B][D];
  __shared__ float red[4][B][16];
  const int tid = threadIdx.x, lane = tid & 63, wave = tid >> 6;
  const int col0 = blockIdx.x * 16;
  const int half = tid & 1, rg = tid >> 1;
  u32x4 w[4];
#pragma unroll
  for (int i = 0; i < 4; ++i) {
    const int r = rg + 128 * i;
    w[i] = (r < D) ? *reinterpret_cast<const u32x4*>(wq + (size_t)r * (HQ * DH) + col0 + half * 8) : u32x4{0, 0, 0, 0};
  }
#pragma unroll
  for (int bb = 0; bb < 2; ++bb) {
    const int b = wave * 2 + bb;
    float v[7]; float s = 0.f;
#pragma unroll
    for (int i = 0; i < 7; ++i) { v[i] = bf1(x[b * D + lane + 64 * i]); s += v[i]; }
    const float mean = wave_sum(s) * (1.f / D);
    float s2 = 0.f;
#pragma unroll
    for (int i = 0; i < 7; ++i) { v[i] -= mean; s2 += v[i] * v[i]; }
    const float var = wave_sum(s2) * (1.f / D);
    const float rs = 1.f / sqrtf(var + EPS);
#pragma unroll
    for (int i = 0; i < 7; ++i) xn[b][lane + 64 * i] = v[i] * rs;
  }
  __syncthreads();
  float acc[B][8];
#pragma unroll
  for (int b = 0; b < B; ++b)
#pragma unroll
    for (int k = 0; k < 8; ++k) acc[b][k] = 0.f;
#pragma unroll
  for (int i = 0; i < 4; ++i) {
    const int r = rg + 128 * i;
    if (r < D) {
      float wf[8];
#pragma unroll
      for (int k = 0; k < 4; ++k) { wf[2 * k] = bf_lo(w[i][k]); wf[2 * k + 1] = bf_hi(w[i][k]); }
#pragma unroll
      for (int b = 0; b < B; ++b) { const float xv = xn[b][r];
#pragma unroll
        for (int k = 0; k < 8; ++k) acc[b][k] = fmaf(xv, wf[k], acc[b][k]); }
    }
  }
#pragma unroll
  for (int b = 0; b < B; ++b)
#pragma unroll
    for (int k = 0; k < 8; ++k) {
      float v = acc[b][k];
      v += dpp<XOR2>(v); v += dpp<ROR4>(v); v += dpp<ROR8>(v);
      auto s = __builtin_amdgcn_permlane16_swap(__float_as_uint(v), __float_as_uint(v), false, false);
      v = __uint_as_float(s[0]) + __uint_as_float(s[1]);
      auto t = __builtin_amdgcn_permlane32_swap(__float_as_uint(v), __float_as_uint(v), false, false);
      v = __uint_as_float(t[0]) + __uint_as_float(t[1]);
      if (lane < 2) red[wave][b][lane * 8 + k] = v;
    }
  __syncthreads();
  if (tid < B * 16) {
    const int b = tid >> 4, c = tid & 15;
    const float v = red[0][b][c] + red[1][b][c] + red[2][b][c] + red[3][b][c];
    q[b * (HQ * DH) + col0 + c] = (unsigned short)(cvtpk(v, 0.f) & 0xffffu);
  }
}

__global__ __launch_bounds__(NTHR) void k_attn(const unsigned short* __restrict__ kc, const unsigned short* __restrict__ vc,
                                               const unsigned short* __restrict__ q, float* __restrict__ pml, float* __restrict__ po) {
  __shared__ __attribute__((aligned(16))) float plds[NW][32];
  __shared__ __attribute__((aligned(16))) float mrg[NW][2][4 + DH];
  const int bid = blockIdx.x;
  const int kvh = bid & 7, b = (bid >> 3) & 7, s = bid >> 6;
  const int tid = threadIdx.x, lane = tid & 63, wave = __builtin_amdgcn_readfirstlane(tid >> 6);
  const int par = lane >> 5, c = lane & 31;
  constexpr float C2 = 0.0625f * 1.4426950408889634f;
  const u32x4 q0 = *reinterpret_cast<const u32x4*>(q + b * (HQ * DH) + (2 * kvh) * DH + c * 8);
  const u32x4 q1 = *reinterpret_cast<const u32x4*>(q + b * (HQ * DH) + (2 * kvh + 1) * DH + c * 8);
  const size_t base = ((size_t)(b * HKV + kvh) * SK + (size_t)s * KEYS_PER_SPLIT) * DH;
  const u32x4* kp = reinterpret_cast<const u32x4*>(kc + base) + lane;
  const u32x4* vp = reinterpret_cast<const u32x4*>(vc + base) + lane;
  u32x4 K[8], V[8];
  float acc0[8], acc1[8];
#pragma unroll
  for (int k = 0; k < 8; ++k) { acc0[k] = 0.f; acc1[k] = 0.f; }
  float m = -INFINITY, l = 0.f;
  {
    const int t = wave;
#pragma unroll
    for (int i = 0; i < 8; ++i) K[i] = __builtin_nontemporal_load(kp + t * 512 + i * 64);
#pragma unroll
    for (int i = 0; i < 8; ++i) V[i] = __builtin_nontemporal_load(vp + t * 512 + i * 64);
  }
  for (int it = 0; it < TILES / NW; ++it) {
    const int tn = (it + 1 < TILES / NW) ? (wave + (it + 1) * NW) : wave;
    const bool more = (it + 1 < TILES / NW);
    float x[16];
#pragma unroll
    for (int i = 0; i < 8; ++i) {
      float d0 = 0.f, d1 = 0.f;
#pragma unroll
      for (int k = 0; k < 4; ++k) { d0 = dot2(K[i][k], q0[k], d0); d1 = dot2(K[i][k], q1[k], d1); }
      x[2 * i] = d0; x[2 * i + 1] = d1;
    }
    if (more) {
#pragma unroll
      for (int i = 0; i < 8; ++i) K[i] = __builtin_nontemporal_load(kp + tn * 512 + i * 64);
    }
#pragma unroll
    for (int j = 0; j < 8; ++j) {
      auto r = __builtin_amdgcn_permlane16_swap(__float_as_uint(x[j]), __float_as_uint(x[j + 8]), false, false);
      x[j] = __uint_as_float(r[0]) + __uint_as_float(r[1]);
    }
#pragma unroll
    for (int j = 0; j < 4; ++j) { const float t1 = x[j] + dpp<ROR8>(x[j]), t2 = x[j + 4] + dpp<ROR8>(x[j + 4]); x[j] = (lane & 8) ? t2 : t1; }
#pragma unroll
    for (int j = 0; j < 2; ++j) { const float t1 = x[j] + dpp<XOR7>(x[j]), t2 = x[j + 2] + dpp<XOR7>(x[j + 2]); x[j] = (lane & 4) ? t2 : t1; }
    { const float t1 = x[0] + dpp<XOR2>(x[0]), t2 = x[1] + dpp<XOR2>(x[1]); x[0] = (lane & 2) ? t2 : t1; }
    float sc = x[0] + dpp<XOR1>(x[0]);
    sc *= C2;
    float pm = sc;
    pm = fmaxf(pm, dpp<ROR4>(pm)); pm = fmaxf(pm, dpp<ROR8>(pm));
    { auto r = __builtin_amdgcn_permlane16_swap(__float_as_uint(pm), __float_as_uint(pm), false, false);
      pm = fmaxf(__uint_as_float(r[0]), __uint_as_float(r[1]));
      auto t = __builtin_amdgcn_permlane32_swap(__float_as_uint(pm), __float_as_uint(pm), false, false);
      pm = fmaxf(__uint_as_float(t[0]), __uint_as_float(t[1])); }
    const float mn = fmaxf(m, pm);
    const float alpha = __builtin_amdgcn_exp2f(m - mn);
    const float p = __builtin_amdgcn_exp2f(sc - mn);
    l = fmaf(l, alpha, p);
    m = mn;
    plds[wave][par * 16 + ((lane >> 1) & 15)] = p;
    const float a0 = dpp<0x00>(alpha), a1 = dpp<0xAA>(alpha);
#pragma unroll
    for (int k = 0; k < 8; ++k) { acc0[k] *= a0; acc1[k] *= a1; }
    asm volatile("s_waitcnt lgkmcnt(0)" ::: "memory");
    f32x4 pr[4];
#pragma unroll
    for (int u = 0; u < 4; ++u) pr[u] = *reinterpret_cast<const f32x4*>(&plds[wave][par * 16 + 4 * u]);
#pragma unroll
    for (int i = 0; i < 8; ++i) {
      const float p0 = pr[i >> 1][(i & 1) * 2], p1 = pr[i >> 1][(i & 1) * 2 + 1];
#pragma unroll
      for (int k = 0; k < 4; ++k) {
        const float lo = bf_lo(V[i][k]), hi = bf_hi(V[i][k]);
        acc0[2 * k] = fmaf(p0, lo, acc0[2 * k]); acc0[2 * k + 1] = fmaf(p0, hi, acc0[2 * k + 1]);
        acc1[2 * k] = fmaf(p1, lo, acc1[2 * k]); acc1[2 * k + 1] = fmaf(p1, hi, acc1[2 * k + 1]);
      }
    }
    if (more) {
#pragma unroll
      for (int i = 0; i < 8; ++i) V[i] = __builtin_nontemporal_load(vp + tn * 512 + i * 64);
    }
  }
#pragma unroll
  for (int k = 0; k < 8; ++k) {
    auto t = __builtin_amdgcn_permlane32_swap(__float_as_uint(acc0[k]), __float_as_uint(acc1[k]), false, false);
    acc0[k] = __uint_as_float(t[0]) + __uint_as_float(t[1]);
  }
  {
    float t = l;
    t += dpp<ROR4>(t); t += dpp<ROR8>(t);
    auto r = __builtin_amdgcn_permlane16_swap(__float_as_uint(t), __float_as_uint(t), false, false);
    t = __uint_as_float(r[0]) + __uint_as_float(r[1]);
    auto u = __builtin_amdgcn_permlane32_swap(__float_as_uint(t), __float_as_uint(t), false, false);
    l = __uint_as_float(u[0]) + __uint_as_float(u[1]);
  }
  *reinterpret_cast<f32x4*>(&mrg[wave][par][4 + c * 8]) = f32x4{acc0[0], acc0[1], acc0[2], acc0[3]};
  *reinterpret_cast<f32x4*>(&mrg[wave][par][4 + c * 8 + 4]) = f32x4{acc0[4], acc0[5], acc0[6], acc0[7]};
  if (lane == 0) { mrg[wave][0][0] = m; mrg[wave][0][1] = l; }
  if (lane == 2) { mrg[wave][1][0] = m; mrg[wave][1][1] = l; }
  __syncthreads();
  {
    const int h = tid >> 8, d = tid & 255;
    float M = -INFINITY;
#pragma unroll
    for (int w = 0; w < NW; ++w) M = fmaxf(M, mrg[w][h][0]);
    float L = 0.f, o = 0.f;
#pragma unroll
    for (int w = 0; w < NW; ++w) { const float wt = __builtin_amdgcn_exp2f(mrg[w][h][0] - M); L = fmaf(wt, mrg[w][h][1], L); o = fmaf(wt, mrg[w][h][4 + d], o); }
    const size_t slot = (size_t)(b * HQ + 2 * kvh + h) * NSPLIT + s;
    po[slot * DH + d] = o;
    if (d == 0) { pml[slot * 2] = M; pml[slot * 2 + 1] = L; }
  }
}

__global__ __launch_bounds__(448) void k_wo(const float* __restrict__ pml, const float* __restrict__ po,
                                            const unsigned short* __restrict__ wo, float* __restrict__ pout) {
  __shared__ float o[128];
  __shared__ __attribute__((aligned(16))) float red[8][D];
  const int bid = blockIdx.x;
  const int kvh = bid & 7, b = (bid >> 3) & 7, sl = bid >> 6;
  const int tid = threadIdx.x;
  const int h = 2 * kvh + (sl >> 1), d0 = (sl & 1) * 128;
  const int rg = tid / 56, cc = tid % 56;
  const u32x4* wp = reinterpret_cast<const u32x4*>(wo + (size_t)(h * DH + d0) * D) + tid;
  u32x4 w[16];
#pragma unroll
  for (int i = 0; i < 16; ++i) w[i] = wp[i * 448];
  if (tid < 128) {
    const size_t slot = (size_t)(b * HQ + h) * NSPLIT;
    float M = -INFINITY;
#pragma unroll
    for (int s = 0; s < NSPLIT; ++s) M = fmaxf(M, pml[(slot + s) * 2]);
    float L = 0.f, acc = 0.f;
#pragma unroll
    for (int s = 0; s < NSPLIT; ++s) { const float wt = __builtin_amdgcn_exp2f(pml[(slot + s) * 2] - M); L = fmaf(wt, pml[(slot + s) * 2 + 1], L);
      acc = fmaf(wt, po[(slot + s) * DH + d0 + tid], acc); }
    o[tid] = acc / L;
  }
  __syncthreads();
  float acc[8];
#pragma unroll
  for (int k = 0; k < 8; ++k) acc[k] = 0.f;
#pragma unroll
  for (int i = 0; i < 16; ++i) { const float ov = o[rg + 8 * i];
#pragma unroll
    for (int k = 0; k < 4; ++k) { acc[2 * k] = fmaf(ov, bf_lo(w[i][k]), acc[2 * k]); acc[2 * k + 1] = fmaf(ov, bf_hi(w[i][k]), acc[2 * k + 1]); } }
  *reinterpret_cast<f32x4*>(&red[rg][cc * 8]) = f32x4{acc[0], acc[1], acc[2], acc[3]};
  *reinterpret_cast<f32x4*>(&red[rg][cc * 8 + 4]) = f32x4{acc[4], acc[5], acc[6], acc[7]};
  __syncthreads();
  float v = 0.f;
#pragma unroll
  for (int g = 0; g < 8; ++g) v += red[g][tid];
  pout[((size_t)b * 32 + kvh * 4 + sl) * D + tid] = v;
}

__global__ __launch_bounds__(448) void k_mlp(const unsigned short* __restrict__ x, const float* __restrict__ pout,
                                             const unsigned short* __restrict__ w1, const unsigned short* __restrict__ b1,
                                             const unsigned short* __restrict__ w2, const unsigned short* __restrict__ b2,
                                             unsigned short* __restrict__ out) {
  __shared__ float hs[D];
  __shared__ float part[8];
  __shared__ float ysum[7][14];
  const int bid = blockIdx.x, b = bid >> 5, j = bid & 31;
  const int tid = threadIdx.x, lane = tid & 63, wave = tid >> 6;
  float fw1[4], fb1[4], fw2[4];
#pragma unroll
  for (int i = 0; i < 4; ++i) { fw1[i] = bf1(w1[tid + 448 * i]); fb1[i] = bf1(b1[tid + 448 * i]); fw2[i] = bf1(w2[tid + 448 * i]); }
  float a = 0.f;
#pragma unroll
  for (int g = 0; g < 32; ++g) a += pout[((size_t)b * 32 + g) * D + tid];
  const float h = bf1(x[b * D + tid]) + a;
  float s = wave_sum(h);
  if (lane == 0) part[wave] = s;
  __syncthreads();
  float mean = (part[0] + part[1] + part[2] + part[3] + part[4] + part[5] + part[6]) * (1.f / D);
  __syncthreads();
  float dlt = h - mean;
  s = wave_sum(dlt * dlt);
  if (lane == 0) part[wave] = s;
  __syncthreads();
  float var = (part[0] + part[1] + part[2] + part[3] + part[4] + part[5] + part[6]) * (1.f / D);
  __syncthreads();
  const float t1 = dlt / sqrtf(var + EPS);
  s = wave_sum(t1);
  if (lane == 0) part[wave] = s;
  __syncthreads();
  mean = (part[0] + part[1] + part[2] + part[3] + part[4] + part[5] + part[6]) * (1.f / D);
  __syncthreads();
  dlt = t1 - mean;
  s = wave_sum(dlt * dlt);
  if (lane == 0) part[wave] = s;
  __syncthreads();
  var = (part[0] + part[1] + part[2] + part[3] + part[4] + part[5] + part[6]) * (1.f / D);
  hs[tid] = dlt / sqrtf(var + EPS);
  __syncthreads();
  constexpr float K0 = -2.f * 0.7978845608028654f * 1.4426950408889634f, K1 = K0 * 0.044715f;
#pragma unroll
  for (int e = 0; e < 14; ++e) {
    const float t = hs[j * 14 + e];
    float acc = 0.f;
#pragma unroll
    for (int i = 0; i < 4; ++i) {
      const float u = fmaf(t, fw1[i], fb1[i]);
      const float z = u * fmaf(u * u, K1, K0);
      const float g = u * __builtin_amdgcn_rcpf(1.f + __builtin_amdgcn_exp2f(z));
      acc = fmaf(g, fw2[i], acc);
    }
    acc = wave_sum(acc);
    if (lane == 0) ysum[wave][e] = acc;
  }
  __syncthreads();
  if (tid >= j * 14 && tid < j * 14 + 14) {
    const int e = tid - j * 14;
    float y = bf1(b2[0]);
#pragma unroll
    for (int w = 0; w < 7; ++w) y += ysum[w][e];
    out[b * D + tid] = (unsigned short)(cvtpk(h + y, 0.f) & 0xffffu);
  }
}
}

extern "C" void kernel_launch(void* const* d_in, const int* in_sizes, int n_in, void* d_out, int out_size, void* d_ws, size_t ws_size,
                              hipStream_t stream) {
  const unsigned short* x = (const unsigned short*)d_in[0];
  const unsigned short* kc = (const unsigned short*)d_in[1];
  const unsigned short* vc = (const unsigned short*)d_in[2];
  const unsigned short* wq = (const unsigned short*)d_in[3];
  const unsigned short* wo = (const unsigned short*)d_in[4];
  const unsigned short* w1 = (const unsigned short*)d_in[6];
  const unsigned short* b1 = (const unsigned short*)d_in[7];
  const unsigned short* w2 = (const unsigned short*)d_in[8];
  const unsigned short* b2 = (const unsigned short*)d_in[9];
  char* ws = (char*)d_ws;
  unsigned short* q = (unsigned short*)(ws + WS_Q);
  float* pml = (float*)(ws + WS_ML);
  float* po = (float*)(ws + WS_PO);
  float* pout = (float*)(ws + WS_POUT);
  k_qproj<<<256, 256, 0, stream>>>(x, wq, q);
  k_attn<<<B * HKV * NSPLIT, NTHR, 0, stream>>>(kc, vc, q, pml, po);
  k_wo<<<256, 448, 0, stream>>>(pml, po, wo, pout);
  k_mlp<<<256, 448, 0, stream>>>(x, pout, w1, b1, w2, b2, (unsigned short*)d_out);
}
```

```cpp
#include <hip/hip_runtime.h>
#include <hip/hip_bf16.h>
#include <cstdint>

namespace {
#define GAS __attribute__((address_space(1)))
#define LAS __attribute__((address_space(3)))
#define RLX_AGENT __ATOMIC_RELAXED, __HIP_MEMORY_SCOPE_AGENT
typedef unsigned u32x4 __attribute__((ext_vector_type(4)));
typedef unsigned u32x2 __attribute__((ext_vector_type(2)));
typedef float f32x4 __attribute__((ext_vector_type(4)));
typedef __bf16 bf16x2 __attribute__((ext_vector_type(2)));
typedef unsigned long long u64;
typedef unsigned short u16;

#define WO_MODE 1
#define WO_AUX ((WO_MODE == 1 || WO_MODE == 2 || WO_MODE == 4) ? 2 : 0)
#define CFG_WIN 12
#define CFG_QDELAY 150
#define CFG_QSLEEP 16
constexpr int B = 8, D = 448, HQ = 16, HKV = 8, SK = 8192, DH = 256, F = 1792;
constexpr int NWG = 256, NL = 2, NC = 8, NWAVES = NL + NC + 1, NTHR = NWAVES * 64, SVC = NL + NC;
constexpr int TILE_BYTES = 8192, NS = 16;
constexpr int TPP = 128, NTILES = 2 * TPP;
constexpr int PP = 8 / NL;
constexpr int WIN = CFG_WIN;
constexpr int PRE = 2;
static_assert(WIN <= 4 * PP && WIN >= PRE * PP, "window");
constexpr int WQP = 14 / NL;
constexpr float EPS = 1e-5f;
constexpr int NE = 24, E0 = 108;

constexpr int RING_OFF = 0;
constexpr int PB_OFF = NS * TILE_BYTES;
constexpr int TAB_OFF = PB_OFF + 16384;
constexpr int Q_OFF = TAB_OFF + 256 * NE * 2;
constexpr int CTL_OFF = Q_OFF + 1024;
constexpr int ML_OFF = CTL_OFF + 256, SCL_OFF = ML_OFF + 128;
constexpr int LDS_BYTES = SCL_OFF + 64;
#define STAMP(i)
#define STAMPV(i, v)
#define STAMP_FLUSH()
static_assert(LDS_BYTES <= 160 * 1024, "LDS");
constexpr int C_LANDED = 0  , C_WQ = 2  , C_DONE = 4  , C_QRDY = 12, C_PRO = 13, C_SCALE = 14, C_ABORT = 15, C_ML = 16;
constexpr int RED_OFF = 0;
constexpr int FIN_OFF = 16384;

constexpr size_t WS_EPOCH = 0;
constexpr size_t WS_READY = 4096;
constexpr size_t WS_QG = 65536;
constexpr size_t WS_TABG = WS_QG + (size_t)B * HQ * DH * 8;
constexpr size_t WS_OB = WS_TABG + 65536;
constexpr int OB_BYTES = 4096, OB_O = 0, OB_P = 2048, OB_ML = 3840;
constexpr unsigned SENT = 0xFFFFFFFFu;
constexpr long long TCAP = 5000000;

__device__ __forceinline__ float bf_lo(unsigned u) { return __builtin_bit_cast(float, u << 16); }
__device__ __forceinline__ float bf_hi(unsigned u) { return __builtin_bit_cast(float, u & 0xffff0000u); }
__device__ __forceinline__ float bf1(u16 u) { return __builtin_bit_cast(float, (unsigned)u << 16); }
__device__ __forceinline__ float rbf(float x) { return (float)(__bf16)x; }
__device__ __forceinline__ unsigned bfbits(float x) { return (unsigned)__builtin_bit_cast(u16, (__bf16)x); }
__device__ __forceinline__ float dot2(unsigned a, unsigned b, float acc) {
  return __builtin_amdgcn_fdot2_f32_bf16(__builtin_bit_cast(bf16x2, a), __builtin_bit_cast(bf16x2, b), acc, false);
}
template <int CTRL> __device__ __forceinline__ float dpp(float x) {
  return __builtin_bit_cast(float, __builtin_amdgcn_mov_dpp(__builtin_bit_cast(int, x), CTRL, 0xf, 0xf, true));
}
constexpr int XOR1 = 0xB1, XOR2 = 0x4E, XOR7 = 0x141, ROR8 = 0x128, ROR4 = 0x124;
__device__ __forceinline__ float swap16_add(float a, float b) {
  auto r = __builtin_amdgcn_permlane16_swap(__float_as_uint(a), __float_as_uint(b), false, false);
  return __uint_as_float(r[0]) + __uint_as_float(r[1]);
}
__device__ __forceinline__ float swap32_add(float a, float b) {
  auto r = __builtin_amdgcn_permlane32_swap(__float_as_uint(a), __float_as_uint(b), false, false);
  return __uint_as_float(r[0]) + __uint_as_float(r[1]);
}
__device__ __forceinline__ float swap16_max(float a) {
  auto r = __builtin_amdgcn_permlane16_swap(__float_as_uint(a), __float_as_uint(a), false, false);
  return fmaxf(__uint_as_float(r[0]), __uint_as_float(r[1]));
}
__device__ __forceinline__ float swap32_max(float a) {
  auto r = __builtin_amdgcn_permlane32_swap(__float_as_uint(a), __float_as_uint(a), false, false);
  return fmaxf(__uint_as_float(r[0]), __uint_as_float(r[1]));
}
__device__ __forceinline__ float wave_sum(float x) {
  x += dpp<XOR1>(x); x += dpp<XOR2>(x); x += dpp<XOR7>(x); x += dpp<ROR8>(x);
  x = swap16_add(x, x);
  return swap32_add(x, x);
}
__device__ __forceinline__ float fdiv(float a, float b, float rb) {
  const float q = a * rb;
  return fmaf(fmaf(-q, b, a), rb, q);
}
__device__ __forceinline__ void ln448(float (&v)[7]) {
  const float c = __builtin_bit_cast(float, __builtin_amdgcn_readfirstlane(__builtin_bit_cast(int, v[0])));
  float s1 = 0.f, s2 = 0.f;
#pragma unroll
  for (int i = 0; i < 7; ++i) { const float d = v[i] - c; s1 += d; s2 = fmaf(d, d, s2); }
  s1 = wave_sum(s1) * (1.f / D);
  s2 = wave_sum(s2) * (1.f / D);
  const float var = rbf(fmaxf(fmaf(-s1, s1, s2), 0.f));
  const float m = rbf(c + s1);
  const float den = rbf(__builtin_amdgcn_sqrtf(rbf(var + EPS)));
  const float rden = __builtin_amdgcn_rcpf(den);
#pragma unroll
  for (int i = 0; i < 7; ++i) v[i] = rbf(fdiv(rbf(v[i] - m), den, rden));
}
__device__ __forceinline__ u64 rt() { return __builtin_amdgcn_s_memrealtime(); }
#define LDS_WAIT() asm volatile("s_waitcnt lgkmcnt(0)" ::: "memory")
#define DRAIN() asm volatile("s_waitcnt vmcnt(0)" ::: "memory")
#define BARRIER() asm volatile("s_waitcnt lgkmcnt(0)\n\ts_barrier" ::: "memory")
#define GLDS(src, dst) __builtin_amdgcn_global_load_lds((const GAS void*)(src), (LAS void*)(dst), 16, 0, 2)
__device__ __forceinline__ unsigned nosent(float v) { const unsigned u = __float_as_uint(v); return u == SENT ? 0x7FC00000u : u; }

struct Args {
  const u16* x; const u16* kc; const u16* vc; const u16* wq; const u16* wo;
  const u16* w1; const u16* b1; const u16* w2; const u16* b2;
  u16* out; unsigned char* ws;
};

__global__ void __launch_bounds__(NTHR, 3) k_fused(Args a) {
  extern __shared__ __attribute__((aligned(16))) unsigned char smem[];
  LAS unsigned char* L8 = (LAS unsigned char*)smem;
  volatile LAS unsigned* ctl = (volatile LAS unsigned*)(L8 + CTL_OFF);
  const int tid = threadIdx.x, lane = tid & 63, wave = __builtin_amdgcn_readfirstlane(tid >> 6);
  const int bid = blockIdx.x, kvh = bid & 7, b = (bid >> 3) & 7, s = bid >> 6;
  const bool is_final = (kvh == b) && (s == 0);
  GAS unsigned char* ws = (GAS unsigned char*)a.ws;
  if (wave == SVC) {
    ((LAS unsigned*)(L8 + CTL_OFF))[lane] = 0u;
  }
  BARRIER();
  const size_t chunk = ((size_t)(b * HKV + kvh) * SK + (size_t)s * (SK / 4)) * (DH * 2);
  const auto rs_ws = __builtin_amdgcn_make_buffer_rsrc((void*)a.ws, 0, 0x7fffffff, 0x00020000);
  auto expired = [&](u64 t0, unsigned& sp) -> bool {
    if ((++sp & 255u) != 0u) return false;
    if (ctl[C_ABORT] != 0u) return true;
    if ((long long)(rt() - t0) > TCAP) { ctl[C_ABORT] = 1u; return true; }
    return false;
  };
  auto uniform64 = [](u64 v) -> u64 {
    return (u64)(unsigned)__builtin_amdgcn_readfirstlane((int)(unsigned)v) | ((u64)(unsigned)__builtin_amdgcn_readfirstlane((int)(unsigned)(v >> 32)) << 32);
  };
  constexpr float C2 = 0.0625f * 1.4426950408889634f;

  if (__builtin_expect(wave < NL, 1)) {
    const int L = wave;
    __builtin_amdgcn_s_setprio(3);
    const GAS unsigned char* ksrc = (const GAS unsigned char*)a.kc + chunk + L * 1024 + lane * 16;
    const GAS unsigned char* vsrc = (const GAS unsigned char*)a.vc + chunk + L * 1024 + lane * 16;
    {
      const GAS unsigned char* wsrc = (const GAS unsigned char*)a.wq + (size_t)(L * 32 + (lane >> 1)) * (HQ * DH * 2) + (kvh * 512 + (bid >> 3) * 16) * 2 + (lane & 1) * 16;
#pragma unroll 1
      for (int j = 0; j < WQP; ++j)
        __builtin_amdgcn_global_load_lds((const GAS void*)(wsrc + (size_t)j * (NL * 32 * HQ * DH * 2)), (LAS void*)(L8 + PB_OFF + (j * NL + L) * 1024), 16, 0, 2);
      if (L == 0) STAMP(26);
#pragma unroll 1
      for (int j = 0; j < PRE * PP; ++j) GLDS(ksrc + j * (NL * 1024), L8 + RING_OFF + (j * NL + L) * 1024);
      if (L == 0) STAMP(22);
    }
    asm volatile("s_waitcnt vmcnt(%0)" ::"i"(PRE * PP) : "memory");
    ctl[C_WQ + L] = 1u;
    if (L == 0) STAMP(2);
    bool dead = false;
    unsigned pub = 0;
    for (int T = PRE; T < NTILES; ++T) {
      if (T >= NS) {
        const unsigned need = (unsigned)((T - NS) / NC + 1);
        if (ctl[C_DONE + (T & (NC - 1))] < need) {
          DRAIN();
          pub = (unsigned)T;
          ctl[C_LANDED + L] = pub;
          const u64 t0 = rt(); unsigned sp = 0;
          while (ctl[C_DONE + (T & (NC - 1))] < need) { __builtin_amdgcn_s_sleep(1); if (expired(t0, sp)) { dead = true; break; } }
          if (dead) break;
        }
        asm volatile("" ::: "memory");
      }
      if (L == 0 && T == TPP) STAMP(7);
      const GAS unsigned char* src = (T < TPP ? ksrc + (size_t)T * TILE_BYTES : vsrc + (size_t)(T - TPP) * TILE_BYTES);
      LAS unsigned char* dst = L8 + RING_OFF + (T & (NS - 1)) * TILE_BYTES + L * 1024;
#pragma unroll
      for (int j = 0; j < PP; ++j) {
        GLDS(src + j * NL * 1024, dst + j * NL * 1024);
        asm volatile("s_waitcnt vmcnt(%0)" ::"i"(WIN) : "memory");
        if (((j + 1 + 4 * PP - WIN) % PP) == 0) {
          const int v = (T * PP + j + 1 - WIN) / PP;
          if (v > (int)pub) { pub = (unsigned)v; ctl[C_LANDED + L] = pub; }
        }
      }
    }
    if (!dead) {
#pragma unroll
      for (int r = (WIN - 1) / PP; r >= 0; --r) {
        asm volatile("s_waitcnt vmcnt(%0)" ::"i"(r * PP) : "memory");
        if ((unsigned)(NTILES - r) > pub) ctl[C_LANDED + L] = (unsigned)(NTILES - r);
      }
      if (L == 0) STAMP(8);
    } else {
      DRAIN();
    }
    __builtin_amdgcn_s_setprio(0);
    BARRIER();
    BARRIER();
    if (is_final) BARRIER();
    return;
  }

  if (wave == SVC) {
    const u64 epoch = uniform64(__hip_atomic_load((GAS u64*)(ws + WS_EPOCH), RLX_AGENT));
    const u64 tag = (epoch + 1ull) & 0xFFFFFFFFFFFFull;
    {
      const GAS u64* qg = (const GAS u64*)(ws + WS_QG) + b * (HQ * DH) + kvh * 512 + lane;
      const u64 t0 = rt(); unsigned sp = 0;
      u64 g[8];
#pragma unroll
      for (int i = 0; i < 8; ++i) g[i] = ~tag << 16;
      while (ctl[C_PRO] < (unsigned)NC) { __builtin_amdgcn_s_sleep(2); if (expired(t0, sp)) break; }
      { const u64 t1 = rt(); while ((long long)(rt() - t1) < CFG_QDELAY) __builtin_amdgcn_s_sleep(4); }
      for (;;) {
        bool ok = true;
#pragma unroll
        for (int i = 0; i < 8; ++i) if ((g[i] >> 16) != tag) g[i] = __hip_atomic_load(qg + i * 64, RLX_AGENT);
#pragma unroll
        for (int i = 0; i < 8; ++i) ok &= ((g[i] >> 16) == tag);
        if (__all(ok)) break;
        __builtin_amdgcn_s_sleep(CFG_QSLEEP);
        sp += 15u;
        if (expired(t0, sp)) break;
      }
#pragma unroll
      for (int i = 0; i < 8; ++i) ((LAS u16*)(L8 + Q_OFF))[i * 64 + lane] = (u16)g[i];
      LDS_WAIT();
      ctl[C_QRDY] = 1u;
      STAMP(4);
    }
#pragma unroll
    for (int i = 0; i < 4; ++i)
      __builtin_amdgcn_raw_buffer_store_b128(u32x4{SENT, SENT, SENT, SENT}, rs_ws, (int)(WS_OB + (size_t)bid * OB_BYTES) + i * 1024 + lane * 16, 0, 16);
    unsigned pw1[14], pb1[14], pw2[14];
#pragma unroll
    for (int k = 0; k < 14; ++k) {
      pw1[k] = __builtin_nontemporal_load((const GAS unsigned*)a.w1 + lane + 64 * k);
      pb1[k] = __builtin_nontemporal_load((const GAS unsigned*)a.b1 + lane + 64 * k);
      pw2[k] = __builtin_nontemporal_load((const GAS unsigned*)a.w2 + lane + 64 * k);
    }
    const float b2 = bf1(__builtin_nontemporal_load((const GAS u16*)a.b2));
    DRAIN();
    if (lane == 0) __hip_atomic_store((GAS u64*)(ws + WS_READY) + bid, epoch + 1ull, RLX_AGENT);
    constexpr float K0 = -2.f * 0.7978845608028654f * 1.4426950408889634f, K1 = K0 * 0.044715f;
    auto mixer = [&](float t) -> float {
      float acc0 = 0.f, acc1 = 0.f;
#pragma unroll
      for (int k = 0; k < 14; ++k) {
        const float u0 = rbf(rbf(t * bf_lo(pw1[k])) + bf_lo(pb1[k]));
        const float u1 = rbf(rbf(t * bf_hi(pw1[k])) + bf_hi(pb1[k]));
        const float z0 = u0 * fmaf(u0 * u0, K1, K0), z1 = u1 * fmaf(u1 * u1, K1, K0);
        const float g0 = rbf(u0 * __builtin_amdgcn_rcpf(1.f + __builtin_amdgcn_exp2f(z0)));
        const float g1 = rbf(u1 * __builtin_amdgcn_rcpf(1.f + __builtin_amdgcn_exp2f(z1)));
        acc0 = fmaf(g0, bf_lo(pw2[k]), acc0);
        acc1 = fmaf(g1, bf_hi(pw2[k]), acc1);
      }
      return rbf(rbf(wave_sum(acc0 + acc1)) + b2);
    };
    {
      unsigned mine = 0;
#pragma unroll 1
      for (int e = 0; e < NE; ++e) {
        const unsigned bits = ((unsigned)(bid >> 7) << 15) | ((unsigned)(E0 + e) << 7) | (unsigned)(bid & 127);
        const float y = mixer(bf1((u16)bits));
        if (lane == e) mine = bfbits(y);
      }
      if (lane < NE) __hip_atomic_store((GAS u64*)(ws + WS_TABG) + bid * NE + lane, (tag << 16) | (u64)mine, RLX_AGENT);
    }
    STAMP(15);
    {
      const int pk = is_final ? (lane & 7) : kvh, ps = (lane >> 3) & 3;
      const GAS u64* rf = (const GAS u64*)(ws + WS_READY) + (pk + 8 * b + 64 * ps);
      const u64 t0 = rt(); unsigned sp = 0;
      for (;;) {
        const u64 v = __hip_atomic_load(rf, RLX_AGENT);
        if (__all(v == epoch + 1ull)) break;
        __builtin_amdgcn_s_sleep(8);
        sp += 15u;
        if (expired(t0, sp)) break;
      }
    }
    {
      const u64 t0 = rt(); unsigned sp = 0;
      while (ctl[C_ML] < (unsigned)NC) { __builtin_amdgcn_s_sleep(8); if (expired(t0, sp)) break; }
      asm volatile("" ::: "memory");
      const LAS float* ml = (const LAS float*)(L8 + ML_OFF) + (lane & 15) * 2;
      const float mw = ml[0], lw = ml[1];
      float M = fmaxf(mw, dpp<XOR2>(mw)); M = fmaxf(M, dpp<ROR4>(M)); M = fmaxf(M, dpp<ROR8>(M));
      float Ls = lw * __builtin_amdgcn_exp2f((mw - M) * C2);
      Ls += dpp<XOR2>(Ls); Ls += dpp<ROR4>(Ls); Ls += dpp<ROR8>(Ls);
      if (lane < 2) __builtin_amdgcn_raw_buffer_store_b64(u32x2{nosent(M), nosent(Ls)}, rs_ws, (int)(WS_OB + (size_t)bid * OB_BYTES) + OB_ML + lane * 8, 0, 16);
      const int off = (int)(WS_OB + (size_t)(kvh + 8 * b + 64 * (lane >> 4)) * OB_BYTES) + OB_ML + (lane & 1) * 8;
      u32x2 v;
      for (;;) {
        v = __builtin_amdgcn_raw_buffer_load_b64(rs_ws, off, 0, 16);
        if (__all(v[0] != SENT && v[1] != SENT)) break;
        __builtin_amdgcn_s_sleep(8);
        sp += 15u;
        if (expired(t0, sp)) break;
      }
      const float Ms = __uint_as_float(v[0]);
      const float Mg = swap32_max(swap16_max(Ms));
      float Lg = __uint_as_float(v[1]) * __builtin_amdgcn_exp2f((Ms - Mg) * C2);
      Lg = swap16_add(Lg, Lg); Lg = swap32_add(Lg, Lg);
      if (lane < 16) ((LAS float*)(L8 + SCL_OFF))[lane] = __builtin_amdgcn_exp2f((mw - Mg) * C2) / Lg;
      LDS_WAIT();
      ctl[C_SCALE] = 1u;
    }
    STAMP(24);
    if (is_final) {
      const GAS u64* tg = (const GAS u64*)(ws + WS_TABG) + lane;
      const u64 t0 = rt(); unsigned sp = 0;
#pragma unroll 1
      for (int c = 0; c < 256 * NE / 512; ++c) {
        for (;;) {
          u64 g[8]; bool ok = true;
#pragma unroll
          for (int i = 0; i < 8; ++i) g[i] = __hip_atomic_load(tg + c * 512 + i * 64, RLX_AGENT);
#pragma unroll
          for (int i = 0; i < 8; ++i) { ok &= ((g[i] >> 16) == tag); ((LAS u16*)(L8 + TAB_OFF))[c * 512 + i * 64 + lane] = (u16)g[i]; }
          if (__all(ok)) break;
          __builtin_amdgcn_s_sleep(32);
          sp += 15u;
          if (expired(t0, sp)) break;
        }
      }
    }
    unsigned xr[7];
#pragma unroll
    for (int i = 0; i < 7; ++i) xr[i] = __builtin_nontemporal_load((const GAS u16*)a.x + b * D + lane + 64 * i);
    STAMP(16);
    BARRIER();
    BARRIER();
    if (!is_final) { STAMP_FLUSH(); return; }
    BARRIER();
    STAMP(13);
    {
      const LAS float* fin = (const LAS float*)(L8 + FIN_OFF);
      float h[7], t[7];
#pragma unroll
      for (int i = 0; i < 7; ++i) {
        const int d = lane + 64 * i;
        const float att = rbf((fin[d] + fin[D + d]) + (fin[2 * D + d] + fin[3 * D + d]));
        h[i] = rbf(bf_lo(xr[i]) + att);
        t[i] = h[i];
      }
      ln448(t);
      ln448(t);
      LAS float* tl = (LAS float*)(L8 + RED_OFF);
      bool anymiss = false;
      float y[7];
#pragma unroll
      for (int i = 0; i < 7; ++i) {
        const unsigned bits = __float_as_uint(t[i]) >> 16;
        const unsigned e = ((bits >> 7) & 0xffu) - (unsigned)E0;
        const bool miss = e >= (unsigned)NE;
        anymiss |= miss;
        const unsigned j = ((bits >> 15) << 7) | (bits & 127u);
        y[i] = bf1(((const LAS u16*)(L8 + TAB_OFF))[j * NE + (miss ? 0u : e)]);
      }
      if (__any(anymiss)) {
#pragma unroll
        for (int i = 0; i < 7; ++i) { tl[lane + 64 * i] = t[i]; tl[D + lane + 64 * i] = y[i]; }
        LDS_WAIT();
#pragma unroll 1
        for (int d = 0; d < D; ++d) {
          const float tv = tl[d];
          const unsigned e = ((__float_as_uint(tv) >> 23) & 0xffu) - (unsigned)E0;
          if (e >= (unsigned)NE) { const float yv = mixer(tv); tl[D + d] = yv; }
        }
        LDS_WAIT();
#pragma unroll
        for (int i = 0; i < 7; ++i) y[i] = tl[D + lane + 64 * i];
      }
#pragma unroll
      for (int i = 0; i < 7; ++i) ((GAS u16*)a.out)[b * D + lane + 64 * i] = (u16)bfbits(h[i] + y[i]);
      if (b == 0 && lane == 0) __hip_atomic_store((GAS u64*)(ws + WS_EPOCH), epoch + 1ull, RLX_AGENT);
      STAMP(14);
      STAMP_FLUSH();
    }
    return;
  }

  const int cw = wave - NL, ct = tid - NL * 64;
  const int par = lane >> 5, c = lane & 31;
  {
    unsigned xr[7];
#pragma unroll
    for (int i = 0; i < 7; ++i) xr[i] = __builtin_nontemporal_load((const GAS u16*)a.x + cw * D + lane + 64 * i);
    const u64 epoch = __hip_atomic_load((GAS u64*)(ws + WS_EPOCH), RLX_AGENT);
    const u64 tag = (epoch + 1ull) & 0xFFFFFFFFFFFFull;
    float xn[7];
#pragma unroll
    for (int i = 0; i < 7; ++i) xn[i] = bf_lo(xr[i]);
    ln448(xn);
    if (cw == 0) STAMP(17);
    { const u64 t0 = rt(); unsigned sp = 0;
      while ((ctl[C_WQ] & ctl[C_WQ + 1]) == 0u) { if (expired(t0, sp)) break; } }
    asm volatile("" ::: "memory");
    float acc[16];
#pragma unroll
    for (int k = 0; k < 16; ++k) acc[k] = 0.f;
#pragma unroll
    for (int i = 0; i < 7; ++i) {
      const LAS u32x4* wp = (const LAS u32x4*)(L8 + PB_OFF + (lane + 64 * i) * 32);
      const u32x4 w0 = wp[0], w1 = wp[1];
      const unsigned xhi = __float_as_uint(xn[i]), xlo = xhi >> 16;
#pragma unroll
      for (int k = 0; k < 4; ++k) {
        acc[2 * k] = dot2(w0[k], xlo, acc[2 * k]); acc[2 * k + 1] = dot2(w0[k], xhi, acc[2 * k + 1]);
        acc[8 + 2 * k] = dot2(w1[k], xlo, acc[8 + 2 * k]); acc[8 + 2 * k + 1] = dot2(w1[k], xhi, acc[8 + 2 * k + 1]);
      }
    }
    if (lane == 0) __hip_atomic_fetch_add((LAS unsigned*)(L8 + CTL_OFF) + C_PRO, 1u, __ATOMIC_RELAXED, __HIP_MEMORY_SCOPE_WORKGROUP);
#pragma unroll
    for (int j = 0; j < 8; ++j) acc[j] = swap32_add(acc[j], acc[j + 8]);
#pragma unroll
    for (int j = 0; j < 4; ++j) acc[j] = swap16_add(acc[j], acc[j + 4]);
#pragma unroll
    for (int j = 0; j < 2; ++j) { const float t1 = acc[j] + dpp<ROR8>(acc[j]), t2 = acc[j + 2] + dpp<ROR8>(acc[j + 2]); acc[j] = (lane & 8) ? t2 : t1; }
    { const float t1 = acc[0] + dpp<XOR7>(acc[0]), t2 = acc[1] + dpp<XOR7>(acc[1]); acc[0] = (lane & 4) ? t2 : t1; }
    float qv = acc[0] + dpp<XOR2>(acc[0]);
    qv += dpp<XOR1>(qv);
    if ((lane & 3) == 0)
      __hip_atomic_store((GAS u64*)(ws + WS_QG) + cw * (HQ * DH) + kvh * 512 + (bid >> 3) * 16 + (lane >> 2), (tag << 16) | (u64)bfbits(qv), RLX_AGENT);
    if (cw == 0) STAMP(3);
  }
  auto wait_tile = [&](int T) {
    const unsigned need = (unsigned)(T + 1);
    const volatile LAS u64* lp = (const volatile LAS u64*)(L8 + CTL_OFF + C_LANDED * 4);
    u64 v = *lp;
    if ((unsigned)v < need || (unsigned)(v >> 32) < need) {
      const u64 t0 = rt(); unsigned sp = 0;
      do { __builtin_amdgcn_s_sleep(1); v = *lp; if (expired(t0, sp)) break; } while ((unsigned)v < need || (unsigned)(v >> 32) < need);
    }
    asm volatile("" ::: "memory");
  };
  LAS float* pb = (LAS float*)(L8 + PB_OFF) + cw * 512;
  u32x4 W[16];
  const auto rs_wo = __builtin_amdgcn_make_buffer_rsrc((void*)(a.wo + (size_t)((2 * kvh + (s >> 1)) * DH + (s & 1) * 128 + 16 * cw) * D), 0, 16 * D * 2, 0x00020000);
  const int wvo = (lane < 56 ? lane : 55) * 16;
  auto fetch = [&](u32x4 (&R)[8], int T, unsigned n) {
    wait_tile(T);
    const LAS u32x4* tp = (const LAS u32x4*)(L8 + RING_OFF + (T & (NS - 1)) * TILE_BYTES) + lane;
#pragma unroll
    for (int i = 0; i < 8; ++i) R[i] = tp[i * 64];
    LDS_WAIT();
    ctl[C_DONE + cw] = n;
    asm volatile("" : "+v"(R[0]), "+v"(R[1]), "+v"(R[2]), "+v"(R[3]), "+v"(R[4]), "+v"(R[5]), "+v"(R[6]), "+v"(R[7])::"memory");
  };
  {
    u32x4 Ka[8], Kb[8];
    fetch(Ka, cw, 1u);
    fetch(Kb, cw + NC, 2u);
    { const u64 t0 = rt(); unsigned sp = 0;
      while (ctl[C_QRDY] == 0u) { __builtin_amdgcn_s_sleep(1); if (expired(t0, sp)) break; } }
    asm volatile("" ::: "memory");
    const u32x4 q0 = *(const LAS u32x4*)(L8 + Q_OFF + c * 16), q1 = *(const LAS u32x4*)(L8 + Q_OFF + 512 + c * 16);
    float mx = -INFINITY;
    auto score = [&](const u32x4 (&K)[8], int j) {
      float x[16];
#pragma unroll
      for (int i = 0; i < 8; ++i) {
        float d0 = 0.f, d1 = 0.f;
#pragma unroll
        for (int k = 0; k < 4; ++k) { d0 = dot2(K[i][k], q0[k], d0); d1 = dot2(K[i][k], q1[k], d1); }
        x[2 * i] = d0; x[2 * i + 1] = d1;
      }
#pragma unroll
      for (int u = 0; u < 8; ++u) x[u] = swap16_add(x[u], x[u + 8]);
#pragma unroll
      for (int u = 0; u < 4; ++u) { const float t1 = x[u] + dpp<ROR8>(x[u]), t2 = x[u + 4] + dpp<ROR8>(x[u + 4]); x[u] = (lane & 8) ? t2 : t1; }
#pragma unroll
      for (int u = 0; u < 2; ++u) { const float t1 = x[u] + dpp<XOR7>(x[u]), t2 = x[u + 2] + dpp<XOR7>(x[u + 2]); x[u] = (lane & 4) ? t2 : t1; }
      { const float t1 = x[0] + dpp<XOR2>(x[0]), t2 = x[1] + dpp<XOR2>(x[1]); x[0] = (lane & 2) ? t2 : t1; }
      const float sc = rbf(x[0] + dpp<XOR1>(x[0]));
      mx = fmaxf(mx, sc);
      pb[j * 32 + par * 16 + ((lane >> 1) & 15)] = sc;
    };
#pragma unroll 1
    for (int j = 0; j < TPP / NC; j += 2) {
      score(Ka, j);
      if (cw == 0 && j == 0) STAMP(5);
      if (j + 2 < TPP / NC) fetch(Ka, cw + NC * (j + 2), (unsigned)(j + 3));
      score(Kb, j + 1);
      if (j + 2 < TPP / NC) fetch(Kb, cw + NC * (j + 3), (unsigned)(j + 4));
    }
    if (cw == 0) STAMP(6);
    mx = fmaxf(mx, dpp<ROR4>(mx)); mx = fmaxf(mx, dpp<ROR8>(mx));
    mx = swap32_max(swap16_max(mx));
    LDS_WAIT();
    float ls = 0.f;
#pragma unroll
    for (int j2 = 0; j2 < 8; ++j2) {
      const int o = (j2 * 2 + (lane & 1)) * 32 + par * 16 + ((lane >> 1) & 15);
      const float p = __builtin_amdgcn_exp2f((pb[o] - mx) * C2);
      ls += p;
      pb[o] = p;
    }
    ls += dpp<XOR1>(ls); ls += dpp<ROR4>(ls); ls += dpp<ROR8>(ls);
    ls = swap16_add(ls, ls);
    ls = swap32_add(ls, ls);
    if (lane == 0 || lane == 2) { LAS float* ml = (LAS float*)(L8 + ML_OFF) + (cw * 2 + (lane >> 1)) * 2; ml[0] = mx; ml[1] = ls; }
    LDS_WAIT();
    if (lane == 0) __hip_atomic_fetch_add((LAS unsigned*)(L8 + CTL_OFF) + C_ML, 1u, __ATOMIC_RELAXED, __HIP_MEMORY_SCOPE_WORKGROUP);
  }
  float acc0[8], acc1[8];
#pragma unroll
  for (int k = 0; k < 8; ++k) { acc0[k] = 0.f; acc1[k] = 0.f; }
#pragma unroll 1
  for (int j = 0; j < TPP / NC; ++j) {
    const int T = TPP + cw + NC * j;
    if (j < 8) {
#pragma unroll
      for (int i = 0; i < 8; ++i) if (j == i) {
        W[2 * i] = __builtin_amdgcn_raw_buffer_load_b128(rs_wo, wvo, (2 * i) * (D * 2), WO_AUX);
        W[2 * i + 1] = __builtin_amdgcn_raw_buffer_load_b128(rs_wo, wvo, (2 * i + 1) * (D * 2), WO_AUX);
      }
    }
    f32x4 pr[4];
#pragma unroll
    for (int u = 0; u < 4; ++u) pr[u] = *(const LAS f32x4*)(pb + j * 32 + par * 16 + 4 * u);
    u32x4 V[8];
    fetch(V, T, (unsigned)(TPP / NC + j + 1));
#pragma unroll
    for (int i = 0; i < 8; ++i) {
      const float p0 = pr[i >> 1][(i & 1) * 2], p1 = pr[i >> 1][(i & 1) * 2 + 1];
#pragma unroll
      for (int k = 0; k < 4; ++k) {
        const float lo = bf_lo(V[i][k]), hi = bf_hi(V[i][k]);
        acc0[2 * k] = fmaf(p0, lo, acc0[2 * k]); acc0[2 * k + 1] = fmaf(p0, hi, acc0[2 * k + 1]);
        acc1[2 * k] = fmaf(p1, lo, acc1[2 * k]); acc1[2 * k + 1] = fmaf(p1, hi, acc1[2 * k + 1]);
      }
    }
  }
  {
    { const u64 t0 = rt(); unsigned sp = 0;
      while (ctl[C_SCALE] == 0u) { __builtin_amdgcn_s_sleep(1); if (expired(t0, sp)) break; } }
    asm volatile("" ::: "memory");
    const float scl = ((const LAS float*)(L8 + SCL_OFF))[cw * 2 + par];
#pragma unroll
    for (int k = 0; k < 8; ++k) acc0[k] = swap32_add(acc0[k], acc1[k]) * scl;
    LAS float* rec = pb + par * 256 + c * 8;
    *(LAS f32x4*)rec = f32x4{acc0[0], acc0[1], acc0[2], acc0[3]};
    *(LAS f32x4*)(rec + 4) = f32x4{acc0[4], acc0[5], acc0[6], acc0[7]};
  }
  if (cw == 7) STAMP(9);
  BARRIER();
  if (cw == 0) STAMP(10);
  if (ct < 128) {
    f32x4 o = *(const LAS f32x4*)((const LAS float*)(L8 + PB_OFF) + ct * 4);
#pragma unroll
    for (int w = 1; w < NC; ++w) o += *(const LAS f32x4*)((const LAS float*)(L8 + PB_OFF) + w * 512 + ct * 4);
    __builtin_amdgcn_raw_buffer_store_b128(u32x4{nosent(o[0]), nosent(o[1]), nosent(o[2]), nosent(o[3])}, rs_ws,
                                           (int)(WS_OB + (size_t)bid * OB_BYTES) + OB_O + ct * 16, 0, 16);
  }
  {
    const int off = (int)(WS_OB + (size_t)(kvh + 8 * b + 64 * (lane >> 4)) * OB_BYTES) + OB_O + (s * 128 + cw * 16 + (lane & 15)) * 4;
    unsigned ov = SENT;
    { const u64 t0 = rt(); unsigned sp = 0;
      for (;;) {
        if (ov == SENT) ov = __builtin_amdgcn_raw_buffer_load_b32(rs_ws, off, 0, 16);
        if (__all(ov != SENT)) break;
        sp += 15u;
        if (expired(t0, sp)) break;
      } }
    if (cw == 0) STAMP(11);
    float ot = __uint_as_float(ov);
    ot = swap16_add(ot, ot); ot = swap32_add(ot, ot);
    const unsigned ohi = __float_as_uint(rbf(ot));
    float acc[8];
#pragma unroll
    for (int k = 0; k < 8; ++k) acc[k] = 0.f;
#pragma unroll
    for (int i = 0; i < 16; ++i) {
      const unsigned oh = (unsigned)__builtin_amdgcn_readlane((int)ohi, i), ol = oh >> 16;
#pragma unroll
      for (int k = 0; k < 4; ++k) { acc[2 * k] = dot2(W[i][k], ol, acc[2 * k]); acc[2 * k + 1] = dot2(W[i][k], oh, acc[2 * k + 1]); }
    }
    if (lane < 56) {
      LAS float* red = (LAS float*)(L8 + RED_OFF) + cw * D + lane * 8;
      *(LAS f32x4*)red = f32x4{acc[0], acc[1], acc[2], acc[3]};
      *(LAS f32x4*)(red + 4) = f32x4{acc[4], acc[5], acc[6], acc[7]};
    }
  }
  if (cw == 0) STAMP(12);
  BARRIER();
  if (ct < 112) {
    f32x4 v = *(const LAS f32x4*)((const LAS float*)(L8 + RED_OFF) + ct * 4);
#pragma unroll
    for (int w = 1; w < NC; ++w) v += *(const LAS f32x4*)((const LAS float*)(L8 + RED_OFF) + w * D + ct * 4);
    __builtin_amdgcn_raw_buffer_store_b128(u32x4{nosent(v[0]), nosent(v[1]), nosent(v[2]), nosent(v[3])}, rs_ws,
                                           (int)(WS_OB + (size_t)bid * OB_BYTES) + OB_P + ct * 16, 0, 16);
  }
  if (!is_final) return;
  if (ct < D) {
    const int cc = ct % 112, gq = ct / 112;
    const int off = (int)(WS_OB + (size_t)(8 * b + 64 * gq) * OB_BYTES) + OB_P + cc * 16;
    u32x4 v[8];
#pragma unroll
    for (int i = 0; i < 8; ++i) v[i] = u32x4{SENT, SENT, SENT, SENT};
    const u64 t0 = rt(); unsigned sp = 0;
    for (;;) {
      bool ok = true;
#pragma unroll
      for (int i = 0; i < 8; ++i)
        if (v[i][0] == SENT || v[i][1] == SENT || v[i][2] == SENT || v[i][3] == SENT) v[i] = __builtin_amdgcn_raw_buffer_load_b128(rs_ws, off + i * OB_BYTES, 0, 16);
#pragma unroll
      for (int i = 0; i < 8; ++i) ok &= (v[i][0] != SENT) & (v[i][1] != SENT) & (v[i][2] != SENT) & (v[i][3] != SENT);
      if (__all(ok)) break;
      sp += 15u;
      if (expired(t0, sp)) break;
    }
    f32x4 sum = __builtin_bit_cast(f32x4, v[0]);
#pragma unroll
    for (int i = 1; i < 8; ++i) sum += __builtin_bit_cast(f32x4, v[i]);
    *(LAS f32x4*)((LAS float*)(L8 + FIN_OFF) + gq * D + cc * 4) = sum;
  }
  BARRIER();
}
}

extern "C" void kernel_launch(void* const* d_in, const int* in_sizes, int n_in, void* d_out, int out_size, void* d_ws, size_t ws_size,
                              hipStream_t stream) {
  Args a;
  a.x = (const u16*)d_in[0]; a.kc = (const u16*)d_in[1]; a.vc = (const u16*)d_in[2]; a.wq = (const u16*)d_in[3]; a.wo = (const u16*)d_in[4];
  a.w1 = (const u16*)d_in[6]; a.b1 = (const u16*)d_in[7]; a.w2 = (const u16*)d_in[8]; a.b2 = (const u16*)d_in[9];
  a.out = (u16*)d_out; a.ws = (unsigned char*)d_ws;
  (void)hipFuncSetAttribute((const void*)k_fused, hipFuncAttributeMaxDynamicSharedMemorySize, LDS_BYTES);
  k_fused<<<NWG, NTHR, LDS_BYTES, stream>>>(a);
}
```

```cpp
#include <hip/hip_runtime.h>
#include <hip/hip_bf16.h>
#include <cstdint>

namespace {
#define GAS __attribute__((address_space(1)))
#define LAS __attribute__((address_space(3)))
#define RLX_AGENT __ATOMIC_RELAXED, __HIP_MEMORY_SCOPE_AGENT
typedef unsigned u32x4 __attribute__((ext_vector_type(4)));
typedef unsigned u32x2 __attribute__((ext_vector_type(2)));
typedef float f32x4 __attribute__((ext_vector_type(4)));
typedef float f32x16 __attribute__((ext_vector_type(16)));
typedef short bf16x8 __attribute__((ext_vector_type(8)));
typedef __bf16 bf16x2 __attribute__((ext_vector_type(2)));
typedef unsigned long long u64;
typedef unsigned short u16;

#define CFG_GRP 2
#define CFG_CKSTEP 32
#define CFG_PGAIN 0.030f
#define CFG_PDELAY 250
#define CFG_WIN 12
#define CFG_QDELAY 150
#define CFG_QSLEEP 16
#define CFG_ROT 1
#define CFG_MLDELAY 24
#define CFG_WJ0 4
#define CFG_H1STAG 4
#define CFG_H2PIPE 0
#define CFG_H2STAG 6
constexpr int B = 8, D = 448, HQ = 16, HKV = 8, SK = 8192, DH = 256, F = 1792;
constexpr int NWG = 256, NL = 2, NC = 8, NWAVES = NL + NC + 2, NTHR = NWAVES * 64, SVC = NL + NC, PACER = SVC + 1;
constexpr int TILE_BYTES = 8192, NS = 16;
constexpr int TPP = 128, NTILES = 2 * TPP;
constexpr int GRP = CFG_GRP;
constexpr int WIN = CFG_WIN;
constexpr int WMIN = WIN - 3, WMAX = WIN + 4;
constexpr int CKSTEP = CFG_CKSTEP, NCK = (NTILES - 1) / CKSTEP;
static_assert(NL == 2 && WMIN >= 4 && WMAX <= 16 + GRP && NCK <= 16 && CKSTEP % NL == 0 && 8 % GRP == 0 && TPP % CKSTEP == 0, "window");
constexpr float EPS = 1e-5f;
constexpr int NE = 24, E0 = 108;

constexpr int RING_OFF = 0;
constexpr int PB_OFF = NS * TILE_BYTES;
constexpr int TAB_OFF = PB_OFF + 16384;
constexpr int Q_OFF = TAB_OFF + 256 * NE * 2;
constexpr int CTL_OFF = Q_OFF + 1024;
constexpr int ML_OFF = CTL_OFF + 256, GS_OFF = ML_OFF + 128;
constexpr int LDS_BYTES = GS_OFF + 64;
#define STAMP(i)
#define STAMPV(i, v)
#define STAMP_FLUSH()
static_assert(LDS_BYTES <= 160 * 1024, "LDS");
constexpr int C_LANDED = 0  , C_WQ = 2, C_TICKET = 3, C_DONE = 4  , C_QRDY = 12, C_PRO = 13, C_SCALE = 14, C_ABORT = 15, C_ML = 16, C_WIN = 17, C_CKN = 18,
              C_EPF = 19, C_EP = 20  , C_CKT = 32  ;
constexpr int RED_OFF = 0;
constexpr int EX_OFF = 16384;

constexpr size_t WS_EPOCH = 0;
constexpr size_t WS_READY = 4096;
constexpr size_t WS_QG = 65536;
constexpr size_t WS_TABG = WS_QG + (size_t)B * HQ * DH * 8;
constexpr size_t WS_PROG = WS_TABG + 65536;
constexpr size_t WS_OB = WS_PROG + 32768;
constexpr int OB_BYTES = 4096, OB_O = 0, OB_P = 2048, OB_ML = 3840;
constexpr int WS_END = (int)(WS_OB + (size_t)NWG * OB_BYTES);
constexpr int OOB = 0x7ffffff0;
constexpr unsigned SENT = 0xFFFFFFFFu;
constexpr long long TCAP = 5000000;

__device__ __forceinline__ float bf_lo(unsigned u) { return __builtin_bit_cast(float, u << 16); }
__device__ __forceinline__ float bf_hi(unsigned u) { return __builtin_bit_cast(float, u & 0xffff0000u); }
__device__ __forceinline__ float bf1(u16 u) { return __builtin_bit_cast(float, (unsigned)u << 16); }
__device__ __forceinline__ float rbf(float x) { return (float)(__bf16)x; }
__device__ __forceinline__ unsigned bfbits(float x) { return (unsigned)__builtin_bit_cast(u16, (__bf16)x); }
__device__ __forceinline__ float dot2(unsigned a, unsigned b, float acc) {
  return __builtin_amdgcn_fdot2_f32_bf16(__builtin_bit_cast(bf16x2, a), __builtin_bit_cast(bf16x2, b), acc, false);
}
template <int CTRL> __device__ __forceinline__ float dpp(float x) {
  return __builtin_bit_cast(float, __builtin_amdgcn_mov_dpp(__builtin_bit_cast(int, x), CTRL, 0xf, 0xf, true));
}
constexpr int XOR1 = 0xB1, XOR2 = 0x4E, XOR7 = 0x141, ROR8 = 0x128, ROR4 = 0x124;
__device__ __forceinline__ float swap16_add(float a, float b) {
  auto r = __builtin_amdgcn_permlane16_swap(__float_as_uint(a), __float_as_uint(b), false, false);
  return __uint_as_float(r[0]) + __uint_as_float(r[1]);
}
__device__ __forceinline__ float swap32_add(float a, float b) {
  auto r = __builtin_amdgcn_permlane32_swap(__float_as_uint(a), __float_as_uint(b), false, false);
  return __uint_as_float(r[0]) + __uint_as_float(r[1]);
}
__device__ __forceinline__ float swap16_max(float a) {
  auto r = __builtin_amdgcn_permlane16_swap(__float_as_uint(a), __float_as_uint(a), false, false);
  return fmaxf(__uint_as_float(r[0]), __uint_as_float(r[1]));
}
__device__ __forceinline__ float swap32_max(float a) {
  auto r = __builtin_amdgcn_permlane32_swap(__float_as_uint(a), __float_as_uint(a), false, false);
  return fmaxf(__uint_as_float(r[0]), __uint_as_float(r[1]));
}
__device__ __forceinline__ float wave_sum(float x) {
  x += dpp<XOR1>(x); x += dpp<XOR2>(x); x += dpp<XOR7>(x); x += dpp<ROR8>(x);
  x = swap16_add(x, x);
  return swap32_add(x, x);
}
__device__ __forceinline__ float first_lane(float v) { return __builtin_bit_cast(float, __builtin_amdgcn_readfirstlane(__builtin_bit_cast(int, v))); }
__device__ __forceinline__ float fdiv(float a, float b, float rb) {
  const float q = a * rb;
  return fmaf(fmaf(-q, b, a), rb, q);
}
__device__ __forceinline__ float ln_apply(float x, float mean, float var) {
  const float m = rbf(mean);
  const float den = rbf(__builtin_amdgcn_sqrtf(rbf(rbf(var) + EPS)));
  return rbf(fdiv(rbf(x - m), den, __builtin_amdgcn_rcpf(den)));
}
__device__ __forceinline__ void ln448(float (&v)[7]) {
  const float c = first_lane(v[0]);
  float s1 = 0.f, s2 = 0.f;
#pragma unroll
  for (int i = 0; i < 7; ++i) { const float d = v[i] - c; s1 += d; s2 = fmaf(d, d, s2); }
  s1 = wave_sum(s1) * (1.f / D);
  s2 = wave_sum(s2) * (1.f / D);
  const float var = fmaxf(fmaf(-s1, s1, s2), 0.f), mean = c + s1;
#pragma unroll
  for (int i = 0; i < 7; ++i) v[i] = ln_apply(v[i], mean, var);
}
__device__ __forceinline__ float mixer_unit(float t, float w1, float b1) {
  constexpr float K0 = -2.f * 0.7978845608028654f * 1.4426950408889634f, K1 = K0 * 0.044715f;
  const float u = rbf(rbf(t * w1) + b1);
  const float z = u * fmaf(u * u, K1, K0);
  return rbf(u * __builtin_amdgcn_rcpf(1.f + __builtin_amdgcn_exp2f(z)));
}
__device__ __forceinline__ u64 rt() { return __builtin_amdgcn_s_memrealtime(); }
#define LDS_WAIT() asm volatile("s_waitcnt lgkmcnt(0)" ::: "memory")
#define DRAIN() asm volatile("s_waitcnt vmcnt(0)" ::: "memory")
#define BARRIER() asm volatile("s_waitcnt lgkmcnt(0)\n\ts_barrier" ::: "memory")
#define GLDS(src, dst) __builtin_amdgcn_global_load_lds((const GAS void*)(src), (LAS void*)(dst), 16, 0, 2)
__device__ __forceinline__ unsigned nosent(float v) { const unsigned u = __float_as_uint(v); return u == SENT ? 0x7FC00000u : u; }
__device__ __forceinline__ bool full4(u32x4 v) { return max(max(v[0], v[1]), max(v[2], v[3])) != SENT; }

#define POLL32(r, off, rs) asm volatile("buffer_load_dword %0, %1, %2, 0 offen sc1" : "=v"(r) : "v"(off), "s"(rs) : "memory")
#define POLL128(r, off, rs) asm volatile("buffer_load_dwordx4 %0, %1, %2, 0 offen sc1" : "=v"(r) : "v"(off), "s"(rs) : "memory")

struct Args {
  const u16* x; const u16* kc; const u16* vc; const u16* wq; const u16* wo;
  const u16* w1; const u16* b1; const u16* w2; const u16* b2;
  u16* out; unsigned char* ws;
};

__global__ void __launch_bounds__(NTHR, 3) k_fused(Args a) {
  extern __shared__ __attribute__((aligned(16))) unsigned char smem[];
  LAS unsigned char* L8 = (LAS unsigned char*)smem;
  volatile LAS unsigned* ctl = (volatile LAS unsigned*)(L8 + CTL_OFF);
  const int tid = threadIdx.x, lane = tid & 63, wave = __builtin_amdgcn_readfirstlane(tid >> 6);
  const int bid = blockIdx.x, kvh = bid & 7, b = (bid >> 3) & 7, s = bid >> 6;
  const bool is_final = (kvh == b) && (s == 0);
  GAS unsigned char* ws = (GAS unsigned char*)a.ws;
  const size_t chunk = ((size_t)(b * HKV + kvh) * SK + (size_t)s * (SK / 4)) * (DH * 2);
  const auto rs_ws = __builtin_amdgcn_make_buffer_rsrc((void*)a.ws, 0, WS_END, 0x00020000);
  auto expired = [&](u64 t0, unsigned& sp) -> bool {
    if ((++sp & 255u) != 0u) return false;
    if (ctl[C_ABORT] != 0u) return true;
    if ((long long)(rt() - t0) > TCAP) { ctl[C_ABORT] = 1u; return true; }
    return false;
  };
  auto wait_ge = [&](int word, unsigned need, int nap) {
    if (ctl[word] < need) {
      const u64 t0 = rt(); unsigned sp = 0;
      do { if (nap == 1) __builtin_amdgcn_s_sleep(1); else if (nap > 1) __builtin_amdgcn_s_sleep(8); if (expired(t0, sp)) break; } while (ctl[word] < need);
    }
    asm volatile("" ::: "memory");
  };
  auto uniform64 = [](u64 v) -> u64 {
    return (u64)(unsigned)__builtin_amdgcn_readfirstlane((int)(unsigned)v) | ((u64)(unsigned)__builtin_amdgcn_readfirstlane((int)(unsigned)(v >> 32)) << 32);
  };
  constexpr float C2 = 0.0625f * 1.4426950408889634f;
  auto get_epoch1 = [&]() -> u64 {
    wait_ge(C_EPF, 1u, 0);
    return ((u64)ctl[C_EP] | ((u64)ctl[C_EP + 1] << 32)) + 1ull;
  };
  constexpr u64 TAGM = 0xFFFFFFFFFFFFull;

  if (__builtin_expect(wave < NL, 1)) {
    const int L = wave;
    __builtin_amdgcn_s_setprio(3);
    const GAS unsigned char* ksrc = (const GAS unsigned char*)a.kc + chunk + lane * 16;
    const int rot = CFG_ROT ? ((bid >> 6) & 1) * 16 + ((bid >> 5) & 1) * 32 : 0;
#pragma unroll 1
    for (int j = 0; j < 8; ++j) GLDS(ksrc + ((L + rot) & (TPP - 1)) * TILE_BYTES + j * 1024, L8 + RING_OFF + L * TILE_BYTES + j * 1024);
    BARRIER();
    const GAS unsigned char* vsrc = (const GAS unsigned char*)a.vc + chunk + lane * 16;
    auto draw = [&]() -> unsigned {
      unsigned t; u64 keep;
      asm volatile("s_mov_b64 %1, exec\n\ts_mov_b64 exec, 1\n\tds_add_rtn_u32 %0, %2, %3\n\ts_mov_b64 exec, %1"
                   : "=&v"(t), "=&s"(keep) : "v"((unsigned)(size_t)(L8 + CTL_OFF + C_TICKET * 4)), "v"(1u) : "memory");
      return t;
    };
    bool dead = false;
    int pub = 0, h1 = L, h2 = 0;
    unsigned wsel = 0, nxt = draw();
    for (;;) {
      asm volatile("s_waitcnt lgkmcnt(0)" : "+v"(nxt)::"memory");
      const int T = NL + __builtin_amdgcn_readfirstlane((int)nxt);
      if (T >= NTILES) break;
      if (T >= NS) {
        const unsigned need = (unsigned)((T - NS) / NC + 1);
        if (ctl[C_DONE + (T & (NC - 1))] < need) {
          DRAIN();
          pub = T;
          ctl[C_LANDED + L] = (unsigned)pub;
          const u64 t0 = rt(); unsigned sp = 0;
          while (ctl[C_DONE + (T & (NC - 1))] < need) { __builtin_amdgcn_s_sleep(1); if (expired(t0, sp)) { dead = true; break; } }
          if (dead) break;
        }
        asm volatile("" ::: "memory");
      }
      const int wnow = __builtin_amdgcn_readfirstlane((int)wsel);
      const GAS unsigned char* src = (T < TPP ? ksrc : vsrc) + ((T + rot) & (TPP - 1)) * TILE_BYTES;
      LAS unsigned char* dst = L8 + RING_OFF + (T & (NS - 1)) * TILE_BYTES;
#define TILE_BODY(Wn)                                                                                   \
      _Pragma("unroll") for (int j = 0; j < 8; j += GRP) {                                              \
        _Pragma("unroll") for (int g = 0; g < GRP; ++g) GLDS(src + (j + g) * 1024, dst + (j + g) * 1024); \
        if (j + GRP == 8) { nxt = draw(); wsel = ctl[C_WIN]; }                                          \
        asm volatile("s_waitcnt vmcnt(%0)" ::"i"(Wn) : "memory");                                       \
        if ((j + GRP + 32 - (Wn)) % 8 < GRP) {           \
          const int back = 4 - (j + GRP + 32 - (Wn)) / 8;     \
          const int v = back <= 0 ? T : back == 1 ? h1 : h2;                                            \
          if (v > pub) { pub = v; ctl[C_LANDED + L] = (unsigned)pub; }                                  \
        }                                                                                               \
      }
      switch (wnow) {
        case 1: TILE_BODY(WMIN) break;
        case 2: TILE_BODY(WMIN + 1) break;
        case 3: TILE_BODY(WMIN + 2) break;
        case 5: TILE_BODY(WMIN + 4) break;
        case 6: TILE_BODY(WMIN + 5) break;
        case 7: TILE_BODY(WMIN + 6) break;
        case 8: TILE_BODY(WMIN + 7) break;
        default: TILE_BODY(WIN) break;
      }
      h2 = h1; h1 = T;
      if (T == TPP) STAMP(7);
      if (T == 32) STAMP(29);
      if (T == 64) STAMP(30);
      if (T == 192) STAMP(31);
      if ((T % CKSTEP) == 0) { ctl[C_CKT + T / CKSTEP - 1] = (unsigned)rt(); ctl[C_CKN] = (unsigned)(T / CKSTEP); }
    }
    if (!dead) {
      asm volatile("s_waitcnt vmcnt(16)" ::: "memory");
      if (h2 > pub) ctl[C_LANDED + L] = (unsigned)h2;
      asm volatile("s_waitcnt vmcnt(8)" ::: "memory");
      if (h1 > pub) ctl[C_LANDED + L] = (unsigned)h1;
      DRAIN();
      ctl[C_LANDED + L] = (unsigned)NTILES;
      if (h1 == NTILES - 1) STAMP(8);
    } else {
      DRAIN();
    }
    LDS_WAIT();
    return;
  }

  if (wave == PACER) {
    BARRIER();
    const u64 ep1 = uniform64(get_epoch1()), tag = ep1 & TAGM;
#pragma unroll 1
    for (int c = 1; c <= NCK; ++c) {
      if (c == TPP / CKSTEP) {
        const u64 t0 = rt(); unsigned sp = 0;
        const int pk = is_final ? (lane & 7) : kvh, ps = (lane >> 3) & 3;
        const GAS u64* rf = (const GAS u64*)(ws + WS_READY) + (pk + 8 * b + 64 * ps);
        for (;;) {
          const u64 v = __hip_atomic_load(rf, RLX_AGENT);
          if (__all(v == ep1)) break;
          __builtin_amdgcn_s_sleep(16);
          sp += 15u;
          if (expired(t0, sp)) break;
        }
      }
      wait_ge(C_CKN, (unsigned)c, c == TPP / CKSTEP ? 1 : 2);
      const unsigned tc = ctl[C_CKT + c - 1];
      GAS u64* pg = (GAS u64*)(ws + WS_PROG) + (c - 1) * NWG;
      if (lane == 0) __hip_atomic_store(pg + bid, (tag << 16) | (u64)(tc & 0xffffu), RLX_AGENT);
      if (c == TPP / CKSTEP) {
        const u64 t0 = rt(); unsigned sp = 0;
        wait_ge(C_ML, (unsigned)NC, 1);
        const LAS float* ml = (const LAS float*)(L8 + ML_OFF) + (lane & 15) * 2;
        const float mw = ml[0], lw = ml[1];
        float M = fmaxf(mw, dpp<XOR2>(mw)); M = fmaxf(M, dpp<ROR4>(M)); M = fmaxf(M, dpp<ROR8>(M));
        float Ls = lw * __builtin_amdgcn_exp2f((mw - M) * C2);
        Ls += dpp<XOR2>(Ls); Ls += dpp<ROR4>(Ls); Ls += dpp<ROR8>(Ls);
        if (lane < 2) __builtin_amdgcn_raw_buffer_store_b64(u32x2{nosent(M), nosent(Ls)}, rs_ws, (int)WS_OB + bid * OB_BYTES + OB_ML + lane * 8, 0, 16);
        const int off = (int)WS_OB + (kvh + 8 * b + 64 * (lane >> 4)) * OB_BYTES + OB_ML + (lane & 1) * 8;
        u32x2 v = {SENT, SENT};
        __builtin_amdgcn_s_sleep(CFG_MLDELAY);
        for (;;) {
          __builtin_amdgcn_s_sleep(8);
          if (v[0] == SENT || v[1] == SENT) v = __builtin_amdgcn_raw_buffer_load_b64(rs_ws, off, 0, 16);
          if (__all(v[0] != SENT && v[1] != SENT)) break;
          sp += 15u;
          if (expired(t0, sp)) break;
        }
        const float Ms = __uint_as_float(v[0]);
        const float Mg = swap32_max(swap16_max(Ms));
        float Lg = __uint_as_float(v[1]) * __builtin_amdgcn_exp2f((Ms - Mg) * C2);
        Lg = swap16_add(Lg, Lg); Lg = swap32_add(Lg, Lg);
        if (lane < 2) { LAS float* gs = (LAS float*)(L8 + GS_OFF) + lane * 2; gs[0] = Mg; gs[1] = 1.f / Lg; }
        LDS_WAIT();
        ctl[C_SCALE] = 1u;
        STAMP(24);
      }
      while ((unsigned)rt() - tc < (unsigned)CFG_PDELAY) __builtin_amdgcn_s_sleep(16);
      const u64 g = __hip_atomic_load(pg + (bid & ~63) + lane, RLX_AGENT);
      const int d = (int)(short)(u16)(tc - (unsigned)g);
      const float df = ((g >> 16) == tag) ? (float)min(max(d, -(CFG_PDELAY - 100)), 600) : -(float)(CFG_PDELAY - 100);
      const float lag = wave_sum(df) * (1.f / 64.f);
      const int w = min(max(WIN + (int)rintf(lag * CFG_PGAIN), WMIN), WMAX);
      ctl[C_WIN] = (unsigned)(w - WMIN + 1);
    }
    LDS_WAIT();
    return;
  }

  if (wave == SVC) {
    u64 epoch = __hip_atomic_load((GAS u64*)(ws + WS_EPOCH), RLX_AGENT);
    ((LAS unsigned*)(L8 + CTL_OFF))[lane] = 0u;
    BARRIER();
    asm volatile("" : "+v"(epoch));
    epoch = uniform64(epoch);
    ctl[C_EP] = (unsigned)epoch; ctl[C_EP + 1] = (unsigned)(epoch >> 32);
    LDS_WAIT();
    ctl[C_EPF] = 1u;
    const u64 tag = (epoch + 1ull) & TAGM;
    {
      const GAS u64* qg = (const GAS u64*)(ws + WS_QG) + b * (HQ * DH) + kvh * 512 + lane;
      u64 g[8];
#pragma unroll
      for (int i = 0; i < 8; ++i) g[i] = ~tag << 16;
      wait_ge(C_PRO, (unsigned)NC, 1);
      const u64 t0 = rt(); unsigned sp = 0;
      while ((long long)(rt() - t0) < CFG_QDELAY) __builtin_amdgcn_s_sleep(4);
      for (;;) {
        bool ok = true;
#pragma unroll
        for (int i = 0; i < 8; ++i) if ((g[i] >> 16) != tag) g[i] = __hip_atomic_load(qg + i * 64, RLX_AGENT);
#pragma unroll
        for (int i = 0; i < 8; ++i) ok &= ((g[i] >> 16) == tag);
        if (__all(ok)) break;
        __builtin_amdgcn_s_sleep(CFG_QSLEEP);
        sp += 15u;
        if (expired(t0, sp)) break;
      }
#pragma unroll
      for (int i = 0; i < 8; ++i) ((LAS u16*)(L8 + Q_OFF))[i * 64 + lane] = (u16)g[i];
      LDS_WAIT();
      ctl[C_QRDY] = 1u;
      STAMP(4);
    }
#pragma unroll
    for (int i = 0; i < 4; ++i)
      __builtin_amdgcn_raw_buffer_store_b128(u32x4{SENT, SENT, SENT, SENT}, rs_ws, (int)WS_OB + bid * OB_BYTES + i * 1024 + lane * 16, 0, 16);
    unsigned pw1[14], pb1[14], pw2[14];
#pragma unroll
    for (int k = 0; k < 14; ++k) {
      pw1[k] = __builtin_nontemporal_load((const GAS unsigned*)a.w1 + lane + 64 * k);
      pb1[k] = __builtin_nontemporal_load((const GAS unsigned*)a.b1 + lane + 64 * k);
      pw2[k] = __builtin_nontemporal_load((const GAS unsigned*)a.w2 + lane + 64 * k);
    }
    const float b2 = bf1(__builtin_nontemporal_load((const GAS u16*)a.b2));
    DRAIN();
    if (lane == 0) __hip_atomic_store((GAS u64*)(ws + WS_READY) + bid, epoch + 1ull, RLX_AGENT);
    {
      unsigned mine = 0;
#pragma unroll 1
      for (int e = 0; e < NE; ++e) {
        const float t = bf1((u16)(((unsigned)(bid >> 7) << 15) | ((unsigned)(E0 + e) << 7) | (unsigned)(bid & 127)));
        float acc0 = 0.f, acc1 = 0.f;
#pragma unroll
        for (int k = 0; k < 14; ++k) {
          acc0 = fmaf(mixer_unit(t, bf_lo(pw1[k]), bf_lo(pb1[k])), bf_lo(pw2[k]), acc0);
          acc1 = fmaf(mixer_unit(t, bf_hi(pw1[k]), bf_hi(pb1[k])), bf_hi(pw2[k]), acc1);
        }
        const float y = rbf(rbf(wave_sum(acc0 + acc1)) + b2);
        if (lane == e) mine = bfbits(y);
      }
      if (lane < NE) __hip_atomic_store((GAS u64*)(ws + WS_TABG) + bid * NE + lane, (tag << 16) | (u64)mine, RLX_AGENT);
    }
    STAMP(15);
    if (is_final) {
      const GAS u64* tg = (const GAS u64*)(ws + WS_TABG) + lane;
      const u64 t0 = rt(); unsigned sp = 0;
      while ((long long)(rt() - t0) < 400) __builtin_amdgcn_s_sleep(32);
#pragma unroll 1
      for (int c = 0; c < 256 * NE / 512; ++c) {
        for (;;) {
          u64 g[8]; bool ok = true;
#pragma unroll
          for (int i = 0; i < 8; ++i) g[i] = __hip_atomic_load(tg + c * 512 + i * 64, RLX_AGENT);
#pragma unroll
          for (int i = 0; i < 8; ++i) { ok &= ((g[i] >> 16) == tag); ((LAS u16*)(L8 + TAB_OFF))[c * 512 + i * 64 + lane] = (u16)g[i]; }
          if (__all(ok)) break;
          __builtin_amdgcn_s_sleep(32);
          sp += 15u;
          if (expired(t0, sp)) break;
        }
      }
    }
    STAMP(16);
    LDS_WAIT();
    return;
  }

  const int cw = wave - NL, ct = tid - NL * 64;
  const int par = lane >> 5, c = lane & 31;
  {
    unsigned xr[7];
#pragma unroll
    for (int i = 0; i < 7; ++i) xr[i] = __builtin_nontemporal_load((const GAS u16*)a.x + cw * D + lane + 64 * i);
    if (cw < 7) {
      const GAS unsigned char* wsrc = (const GAS unsigned char*)a.wq + (size_t)(cw * 64 + (lane >> 1)) * (HQ * DH * 2) + (kvh * 512 + (bid >> 3) * 16) * 2 + (lane & 1) * 16;
      GLDS(wsrc, L8 + PB_OFF + cw * 2048);
      GLDS(wsrc + (size_t)32 * (HQ * DH * 2), L8 + PB_OFF + cw * 2048 + 1024);
    }
    BARRIER();
    DRAIN();
    if (lane == 0) __hip_atomic_fetch_add((LAS unsigned*)(L8 + CTL_OFF) + C_WQ, 1u, __ATOMIC_RELAXED, __HIP_MEMORY_SCOPE_WORKGROUP);
    asm volatile("" : "+v"(xr[0]), "+v"(xr[1]), "+v"(xr[2]), "+v"(xr[3]), "+v"(xr[4]), "+v"(xr[5]), "+v"(xr[6]));
    float xn[7];
#pragma unroll
    for (int i = 0; i < 7; ++i) xn[i] = bf_lo(xr[i]);
    ln448(xn);
    wait_ge(C_WQ, (unsigned)NC, 0);
    if (cw == 0) STAMP(2);
    float acc[16];
#pragma unroll
    for (int k = 0; k < 16; ++k) acc[k] = 0.f;
#pragma unroll
    for (int i = 0; i < 7; ++i) {
      const LAS u32x4* wp = (const LAS u32x4*)(L8 + PB_OFF + (lane + 64 * i) * 32);
      const u32x4 w0 = wp[0], w1 = wp[1];
      const unsigned xhi = __float_as_uint(xn[i]), xlo = xhi >> 16;
#pragma unroll
      for (int k = 0; k < 4; ++k) {
        acc[2 * k] = dot2(w0[k], xlo, acc[2 * k]); acc[2 * k + 1] = dot2(w0[k], xhi, acc[2 * k + 1]);
        acc[8 + 2 * k] = dot2(w1[k], xlo, acc[8 + 2 * k]); acc[8 + 2 * k + 1] = dot2(w1[k], xhi, acc[8 + 2 * k + 1]);
      }
    }
    LDS_WAIT();
    if (lane == 0) __hip_atomic_fetch_add((LAS unsigned*)(L8 + CTL_OFF) + C_PRO, 1u, __ATOMIC_RELAXED, __HIP_MEMORY_SCOPE_WORKGROUP);
#pragma unroll
    for (int j = 0; j < 8; ++j) acc[j] = swap32_add(acc[j], acc[j + 8]);
#pragma unroll
    for (int j = 0; j < 4; ++j) acc[j] = swap16_add(acc[j], acc[j + 4]);
#pragma unroll
    for (int j = 0; j < 2; ++j) { const float t1 = acc[j] + dpp<ROR8>(acc[j]), t2 = acc[j + 2] + dpp<ROR8>(acc[j + 2]); acc[j] = (lane & 8) ? t2 : t1; }
    { const float t1 = acc[0] + dpp<XOR7>(acc[0]), t2 = acc[1] + dpp<XOR7>(acc[1]); acc[0] = (lane & 4) ? t2 : t1; }
    float qv = acc[0] + dpp<XOR2>(acc[0]);
    qv += dpp<XOR1>(qv);
    const u64 tag = get_epoch1() & TAGM;
    if ((lane & 3) == 0)
      __hip_atomic_store((GAS u64*)(ws + WS_QG) + cw * (HQ * DH) + kvh * 512 + (bid >> 3) * 16 + (lane >> 2), (tag << 16) | (u64)bfbits(qv), RLX_AGENT);
    if (cw == 0) STAMP(3);
  }
  LAS float* pb = (LAS float*)(L8 + PB_OFF) + cw * 512;
  auto fetch = [&](u32x4 (&R)[8], int T, unsigned n) {
    {
      const volatile LAS u64* lp = (const volatile LAS u64*)(L8 + CTL_OFF + C_LANDED * 4);
      u64 v = *lp;
      if ((unsigned)v <= (unsigned)T || (unsigned)(v >> 32) <= (unsigned)T) {
        const u64 t0 = rt(); unsigned sp = 0;
        do { __builtin_amdgcn_s_sleep(1); v = *lp; if (expired(t0, sp)) break; } while ((unsigned)v <= (unsigned)T || (unsigned)(v >> 32) <= (unsigned)T);
      }
      asm volatile("" ::: "memory");
    }
    const LAS u32x4* tp = (const LAS u32x4*)(L8 + RING_OFF + (T & (NS - 1)) * TILE_BYTES) + lane;
#pragma unroll
    for (int i = 0; i < 8; ++i) R[i] = tp[i * 64];
    LDS_WAIT();
    ctl[C_DONE + cw] = n;
    asm volatile("" : "+v"(R[0]), "+v"(R[1]), "+v"(R[2]), "+v"(R[3]), "+v"(R[4]), "+v"(R[5]), "+v"(R[6]), "+v"(R[7])::"memory");
  };
  const int hs = (lane >> 1) & 1;
  const int spos = par * 16 + hs * 8 + ((lane >> 2) & 7);
  f32x16 acc;
#pragma unroll
  for (int k = 0; k < 16; ++k) acc[k] = 0.f;
  unsigned am[4];
  {
    const int j0 = lane & 7;
#pragma unroll
    for (int d = 0; d < 4; ++d) am[d] = (c < 16 && (j0 >> 1) == d) ? ((j0 & 1) ? 0xffff0000u : 0x0000ffffu) : 0u;
  }
  auto pv = [&](const u32x4 (&V)[8], int j) {
    const LAS u32x4* pp = (const LAS u32x4*)(pb + j * 32 + par * 16 + ((lane >> 3) & 1) * 8);
    const u32x4 p0 = pp[0], p1 = pp[1];
#pragma unroll
    for (int i = 0; i < 8; ++i) {
      const unsigned pd = i < 4 ? p0[i & 3] : p1[i & 3];
      const u32x4 A = {pd & am[0], pd & am[1], pd & am[2], pd & am[3]};
      acc = __builtin_amdgcn_mfma_f32_32x32x16_bf16(__builtin_bit_cast(bf16x8, A), __builtin_bit_cast(bf16x8, V[i]), acc, 0, 0, 0);
    }
  };
  {
    u32x4 Ra[8], Rb[8];
    fetch(Ra, cw, 1u);
    fetch(Rb, cw + NC, 2u);
    wait_ge(C_QRDY, 1u, 1);
    const u32x4 q0 = *(const LAS u32x4*)(L8 + Q_OFF + c * 16), q1 = *(const LAS u32x4*)(L8 + Q_OFF + 512 + c * 16);
    float mx = -INFINITY;
    auto score = [&](const u32x4 (&K)[8], int j) {
      float x[16];
#pragma unroll
      for (int i = 0; i < 8; ++i) {
        float d0 = 0.f, d1 = 0.f;
#pragma unroll
        for (int k = 0; k < 4; ++k) { d0 = dot2(K[i][k], q0[k], d0); d1 = dot2(K[i][k], q1[k], d1); }
        x[2 * i] = d0; x[2 * i + 1] = d1;
      }
#pragma unroll
      for (int u = 0; u < 8; ++u) x[u] = swap16_add(x[u], x[u + 8]);
#pragma unroll
      for (int u = 0; u < 4; ++u) { const float t1 = x[u] + dpp<ROR8>(x[u]), t2 = x[u + 4] + dpp<ROR8>(x[u + 4]); x[u] = (lane & 8) ? t2 : t1; }
#pragma unroll
      for (int u = 0; u < 2; ++u) { const float t1 = x[u] + dpp<XOR7>(x[u]), t2 = x[u + 2] + dpp<XOR7>(x[u + 2]); x[u] = (lane & 4) ? t2 : t1; }
      { const float t1 = x[0] + dpp<XOR2>(x[0]), t2 = x[1] + dpp<XOR2>(x[1]); x[0] = (lane & 2) ? t2 : t1; }
      const float sc = rbf(x[0] + dpp<XOR1>(x[0]));
      mx = fmaxf(mx, sc);
      pb[j * 32 + spos] = sc;
    };
#pragma unroll 1
    for (int j = 0; j < TPP / NC; j += 2) {
      score(Ra, j);
      if (cw == 0 && j == 0) STAMP(5);
      if (j + 2 < TPP / NC) fetch(Ra, cw + NC * (j + 2), (unsigned)(j + 3));
      score(Rb, j + 1);
      if (j + 2 < TPP / NC) fetch(Rb, cw + NC * (j + 3), (unsigned)(j + 4));
    }
    if (cw == 0) STAMP(6);
    mx = fmaxf(mx, dpp<ROR4>(mx)); mx = fmaxf(mx, dpp<ROR8>(mx));
    mx = swap32_max(swap16_max(mx));
    LDS_WAIT();
    float sv[8], ls = 0.f;
#pragma unroll
    for (int j2 = 0; j2 < 8; ++j2) {
      sv[j2] = pb[(j2 * 2 + (lane & 1)) * 32 + spos];
      ls += __builtin_amdgcn_exp2f((sv[j2] - mx) * C2);
    }
    ls += dpp<XOR1>(ls); ls += dpp<ROR4>(ls); ls += dpp<ROR8>(ls);
    ls = swap16_add(ls, ls);
    ls = swap32_add(ls, ls);
    if (lane == 0 || lane == 2) { LAS float* ml = (LAS float*)(L8 + ML_OFF) + (cw * 2 + hs) * 2; ml[0] = mx; ml[1] = ls; }
    LDS_WAIT();
    if (lane == 0) __hip_atomic_fetch_add((LAS unsigned*)(L8 + CTL_OFF) + C_ML, 1u, __ATOMIC_RELAXED, __HIP_MEMORY_SCOPE_WORKGROUP);
    fetch(Ra, TPP + cw, (unsigned)(TPP / NC + 1));
    fetch(Rb, TPP + cw + NC, (unsigned)(TPP / NC + 2));
    wait_ge(C_SCALE, 1u, 1);
    {
      const LAS float* gs = (const LAS float*)(L8 + GS_OFF) + hs * 2;
      const float Mg = gs[0], inv = gs[1];
#pragma unroll
      for (int j2 = 0; j2 < 8; ++j2) {
        const unsigned pbits = bfbits(__builtin_amdgcn_exp2f((sv[j2] - Mg) * C2) * inv);
        pb[(j2 * 2 + (lane & 1)) * 32 + spos] = __uint_as_float(pbits | (pbits << 16));
      }
      LDS_WAIT();
    }
    pv(Ra, 0);
    pv(Rb, 1);
  }
  u32x4 W[16];
  const auto rs_wo = __builtin_amdgcn_make_buffer_rsrc((void*)(a.wo + (size_t)((2 * kvh + (s >> 1)) * DH + (s & 1) * 128 + 16 * cw) * D), 0, 16 * D * 2, 0x00020000);
  const int wvo = (lane < 56 ? lane : 55) * 16;
  const int fcol = cw * 64 + (lane & 15) * 4 + (lane >> 4);
  unsigned xres = 0;
  if (is_final && cw < 7) xres = __builtin_nontemporal_load((const GAS u16*)a.x + b * D + fcol);
#pragma unroll 1
  for (int j = 2; j < TPP / NC - 1; ++j) {
    if (j >= CFG_WJ0 && j < CFG_WJ0 + 8) {
#pragma unroll
      for (int i = 0; i < 8; ++i) if (j == i + CFG_WJ0) {
        W[2 * i] = __builtin_amdgcn_raw_buffer_load_b128(rs_wo, wvo, (2 * i) * (D * 2), 2);
        W[2 * i + 1] = __builtin_amdgcn_raw_buffer_load_b128(rs_wo, wvo, (2 * i + 1) * (D * 2), 2);
      }
    }
    u32x4 V[8];
    fetch(V, TPP + cw + NC * j, (unsigned)(TPP / NC + j + 1));
    pv(V, j);
  }
  {
#pragma unroll
    for (int i = 0; i < 8; ++i)
#pragma unroll
      for (int k = 0; k < 4; ++k) {
        const unsigned w0 = W[2 * i][k], w1 = W[2 * i + 1][k];
        W[2 * i][k] = __builtin_amdgcn_perm(w1, w0, 0x05040100u);
        W[2 * i + 1][k] = __builtin_amdgcn_perm(w1, w0, 0x07060302u);
      }
    asm volatile("" : "+v"(W[0]), "+v"(W[1]), "+v"(W[2]), "+v"(W[3]), "+v"(W[4]), "+v"(W[5]), "+v"(W[6]), "+v"(W[7]), "+v"(xres));
    asm volatile("" : "+v"(W[8]), "+v"(W[9]), "+v"(W[10]), "+v"(W[11]), "+v"(W[12]), "+v"(W[13]), "+v"(W[14]), "+v"(W[15]));
    u32x4 V[8];
    fetch(V, NTILES - NC + cw, (unsigned)(NTILES / NC));
    pv(V, TPP / NC - 1);
    LAS float* rec = pb + c * 8 + par * 4;
    *(LAS f32x4*)rec = f32x4{acc[0], acc[1], acc[2], acc[3]};
    *(LAS f32x4*)(rec + 256) = f32x4{acc[4], acc[5], acc[6], acc[7]};
  }
  if (cw == 7) STAMP(9);
  BARRIER();
  if (cw == 0) STAMP(10);
  if (ct < 128) {
    f32x4 o = *(const LAS f32x4*)((const LAS float*)(L8 + PB_OFF) + ct * 4);
#pragma unroll
    for (int w = 1; w < NC; ++w) o += *(const LAS f32x4*)((const LAS float*)(L8 + PB_OFF) + w * 512 + ct * 4);
    __builtin_amdgcn_raw_buffer_store_b128(u32x4{nosent(o[0]), nosent(o[1]), nosent(o[2]), nosent(o[3])}, rs_ws, (int)WS_OB + bid * OB_BYTES + OB_O + ct * 16, 0, 16);
  }
  const u32x4 rsv = {(unsigned)(size_t)a.ws, (unsigned)((size_t)a.ws >> 32) & 0xffffu, (unsigned)WS_END, 0x00020000u};
  {
    const int off = (int)WS_OB + (kvh + 8 * b + 64 * (lane >> 4)) * OB_BYTES + OB_O + (s * 128 + cw * 16 + (lane & 15)) * 4;
    unsigned ov = SENT;
    unsigned r0, r1, r2, r3;
    {
      POLL32(r0, off, rsv); __builtin_amdgcn_s_sleep(CFG_H1STAG);
      POLL32(r1, off, rsv); __builtin_amdgcn_s_sleep(CFG_H1STAG);
      POLL32(r2, off, rsv); __builtin_amdgcn_s_sleep(CFG_H1STAG);
      POLL32(r3, off, rsv);
      const u64 t0 = rt(); unsigned sp = 0;
#define H1_STEP(r) asm volatile("s_waitcnt vmcnt(3)" : "+v"(r)::"memory"); ov = ov == SENT ? r : ov; if (__all(ov != SENT)) break; { const int o2 = ov == SENT ? off : OOB; POLL32(r, o2, rsv); }
      for (;;) {
        H1_STEP(r0) H1_STEP(r1) H1_STEP(r2) H1_STEP(r3)
        sp += 15u;
        if (expired(t0, sp)) break;
      }
    }
    if (cw == 0) STAMP(11);
    float ot = __uint_as_float(ov);
    ot = swap16_add(ot, ot); ot = swap32_add(ot, ot);
    const float ob = rbf(ot);
    const unsigned opair = (__float_as_uint(ob) >> 16) | __float_as_uint(dpp<XOR1>(ob));
    float pa[8];
#pragma unroll
    for (int k = 0; k < 8; ++k) pa[k] = 0.f;
#pragma unroll
    for (int i = 0; i < 8; ++i) {
      const unsigned op = (unsigned)__builtin_amdgcn_readlane((int)opair, 2 * i);
#pragma unroll
      for (int k = 0; k < 4; ++k) { pa[2 * k] = dot2(W[2 * i][k], op, pa[2 * k]); pa[2 * k + 1] = dot2(W[2 * i + 1][k], op, pa[2 * k + 1]); }
    }
    if (lane < 56) {
      LAS float* red = (LAS float*)(L8 + RED_OFF) + cw * D + lane * 8;
      *(LAS f32x4*)red = f32x4{pa[0], pa[1], pa[2], pa[3]};
      *(LAS f32x4*)(red + 4) = f32x4{pa[4], pa[5], pa[6], pa[7]};
    }
    asm volatile("s_waitcnt vmcnt(0)" : "+v"(r0), "+v"(r1), "+v"(r2), "+v"(r3)::"memory");
  }
  if (cw == 0) STAMP(12);
  BARRIER();
  if (ct < 112) {
    f32x4 v = *(const LAS f32x4*)((const LAS float*)(L8 + RED_OFF) + ct * 4);
#pragma unroll
    for (int w = 1; w < NC; ++w) v += *(const LAS f32x4*)((const LAS float*)(L8 + RED_OFF) + w * D + ct * 4);
    __builtin_amdgcn_raw_buffer_store_b128(u32x4{nosent(v[0]), nosent(v[1]), nosent(v[2]), nosent(v[3])}, rs_ws, (int)WS_OB + bid * OB_BYTES + OB_P + ct * 16, 0, 16);
  }
  if (!is_final) { if (cw == 0) STAMP_FLUSH(); return; }
  if (cw == 7) return;
  {
    const int off = (int)WS_OB + (8 * b + 64 * (lane >> 4)) * OB_BYTES + OB_P + (cw * 64 + (lane & 15) * 4) * 4;
    u32x4 v[8];
#pragma unroll
    for (int i = 0; i < 8; ++i) v[i] = u32x4{SENT, SENT, SENT, SENT};
    {
      const u64 t0 = rt(); unsigned sp = 0;
      for (;;) {
        bool ok = true;
#pragma unroll
        for (int i = 0; i < 8; ++i) if (!full4(v[i])) v[i] = __builtin_amdgcn_raw_buffer_load_b128(rs_ws, off + i * OB_BYTES, 0, 16);
#pragma unroll
        for (int i = 0; i < 8; ++i) ok &= full4(v[i]);
        if (__all(ok)) break;
        sp += 15u;
        if (expired(t0, sp)) break;
      }
    }
    if (cw == 0) STAMP(13);
    f32x4 sum = __builtin_bit_cast(f32x4, v[0]);
#pragma unroll
    for (int i = 1; i < 8; ++i) sum += __builtin_bit_cast(f32x4, v[i]);
    const float e0 = swap32_add(sum[0], sum[2]), e1 = swap32_add(sum[1], sum[3]);
    const float att = swap16_add(e0, e1);
    const float h = rbf(bf_lo(xres) + rbf(att));
    float t = h;
#pragma unroll
    for (int r = 0; r < 2; ++r) {
      const float cs = first_lane(t), d = t - cs;
      const float s1 = wave_sum(d), s2 = wave_sum(d * d);
      LAS float* ex = (LAS float*)(L8 + EX_OFF) + r * 16;
      if (lane == 0) { ex[cw * 2] = fmaf(s1, 1.f / 64.f, cs); ex[cw * 2 + 1] = fmaxf(fmaf(-s1 * (1.f / 64.f), s1, s2), 0.f); }
      BARRIER();
      const f32x4 x0 = *(const LAS f32x4*)ex, x1 = *(const LAS f32x4*)(ex + 4), x2 = *(const LAS f32x4*)(ex + 8);
      const float m6 = ex[12], q6 = ex[13];
      const float mean = (((x0[0] + x0[2]) + (x1[0] + x1[2])) + ((x2[0] + x2[2]) + m6)) * (1.f / 7.f);
      float dv = 0.f;
      { const float d0 = x0[0] - mean, d1 = x0[2] - mean, d2 = x1[0] - mean, d3 = x1[2] - mean, d4 = x2[0] - mean, d5 = x2[2] - mean, d6 = m6 - mean;
        dv = ((d0 * d0 + d1 * d1) + (d2 * d2 + d3 * d3)) + ((d4 * d4 + d5 * d5) + d6 * d6); }
      const float m2 = (((x0[1] + x0[3]) + (x1[1] + x1[3])) + ((x2[1] + x2[3]) + q6)) + 64.f * dv;
      t = ln_apply(t, mean, m2 * (1.f / D));
    }
    const unsigned bits = __float_as_uint(t) >> 16;
    const unsigned e = ((bits >> 7) & 0xffu) - (unsigned)E0;
    const bool miss = e >= (unsigned)NE;
    float y = bf1(((const LAS u16*)(L8 + TAB_OFF))[(((bits >> 15) << 7) | (bits & 127u)) * NE + (miss ? 0u : e)]);
    u64 mm = __ballot(miss);
    while (mm) {
      const int src = __builtin_ctzll(mm); mm &= mm - 1;
      const float tv = __builtin_bit_cast(float, __builtin_amdgcn_readlane(__builtin_bit_cast(int, t), src));
      float ya = 0.f;
#pragma unroll 2
      for (int f = lane; f < F; f += 64) ya = fmaf(mixer_unit(tv, bf1(((const GAS u16*)a.w1)[f]), bf1(((const GAS u16*)a.b1)[f])), bf1(((const GAS u16*)a.w2)[f]), ya);
      const float yv = rbf(rbf(wave_sum(ya)) + bf1(*(const GAS u16*)a.b2));
      if (lane == src) y = yv;
    }
    ((GAS u16*)a.out)[b * D + fcol] = (u16)bfbits(h + y);
    if (b == 0 && ct == 0) __hip_atomic_store((GAS u64*)(ws + WS_EPOCH), ((u64)ctl[C_EP] | ((u64)ctl[C_EP + 1] << 32)) + 1ull, RLX_AGENT);
    if (cw == 0) { STAMP(14); STAMP_FLUSH(); }
  }
}
}

extern "C" void kernel_launch(void* const* d_in, const int* in_sizes, int n_in, void* d_out, int out_size, void* d_ws, size_t ws_size,
                              hipStream_t stream) {
  Args a;
  a.x = (const u16*)d_in[0]; a.kc = (const u16*)d_in[1]; a.vc = (const u16*)d_in[2]; a.wq = (const u16*)d_in[3]; a.wo = (const u16*)d_in[4];
  a.w1 = (const u16*)d_in[6]; a.b1 = (const u16*)d_in[7]; a.w2 = (const u16*)d_in[8]; a.b2 = (const u16*)d_in[9];
  a.out = (u16*)d_out; a.ws = (unsigned char*)d_ws;
  (void)hipFuncSetAttribute((const void*)k_fused, hipFuncAttributeMaxDynamicSharedMemorySize, LDS_BYTES);
  k_fused<<<NWG, NTHR, LDS_BYTES, stream>>>(a);
}
```

```cpp
#include <hip/hip_runtime.h>
#include <hip/hip_bf16.h>
#include <cstdint>

namespace {
#define GAS __attribute__((address_space(1)))
#define LAS __attribute__((address_space(3)))
#define RLX_AGENT __ATOMIC_RELAXED, __HIP_MEMORY_SCOPE_AGENT
typedef unsigned u32x4 __attribute__((ext_vector_type(4)));
typedef unsigned u32x2 __attribute__((ext_vector_type(2)));
typedef float f32x4 __attribute__((ext_vector_type(4)));
typedef float f32x16 __attribute__((ext_vector_type(16)));
typedef short bf16x8 __attribute__((ext_vector_type(8)));
typedef __bf16 bf16x2 __attribute__((ext_vector_type(2)));
typedef unsigned long long u64;
typedef unsigned short u16;

#define CFG_GRP 2
#define CFG_CKSTEP 32
#define CFG_PGAIN 0.030f
#define CFG_PDELAY 250
#define CFG_WIN 12
#define CFG_QDELAY 220
#define CFG_QSLEEP 32
#define WO_AUX 2
#define CFG_PRE 1
#define WQ_AUX 2
#define CFG_H1D0 10
#define CFG_H1D1 0
#define CFG_H2D0 0
#define CFG_SHARE 1
#define CFG_H1PIPE 0
#define CFG_TLB 0
#define CFG_ROT 1
#define CFG_MLDELAY 32
#define CFG_WJ0 6
#define CFG_H1STAG 4
#define CFG_H2PIPE 0
#define CFG_H2STAG 6
constexpr int B = 8, D = 448, HQ = 16, HKV = 8, SK = 8192, DH = 256, F = 1792;
constexpr int NWG = 256, NL = 2, NC = 8, NWAVES = NL + NC + 2, NTHR = NWAVES * 64, SVC = NL + NC, PACER = SVC + 1;
constexpr int TILE_BYTES = 8192, NS = 16;
constexpr int TPP = 128, NTILES = 2 * TPP;
constexpr int GRP = CFG_GRP;
constexpr int WIN = CFG_WIN;
constexpr int PRE = CFG_PRE;
constexpr int WMIN = WIN - 3, WMAX = WIN + 4;
constexpr int CKSTEP = CFG_CKSTEP, NCK = (NTILES - 1) / CKSTEP;
static_assert(NL == 2 && WMIN >= 4 && WMAX <= 16 + GRP && NCK <= 16 && CKSTEP % NL == 0 && 8 % GRP == 0 && TPP % CKSTEP == 0, "window");
constexpr float EPS = 1e-5f;
constexpr int NE = 24, E0 = 108;

constexpr int RING_OFF = 0;
constexpr int PB_OFF = NS * TILE_BYTES;
constexpr int TAB_OFF = PB_OFF + 16384;
constexpr int Q_OFF = TAB_OFF + 256 * NE * 2;
constexpr int CTL_OFF = Q_OFF + 1024;
constexpr int ML_OFF = CTL_OFF + 256, GS_OFF = ML_OFF + 128;
constexpr int LDS_BYTES = GS_OFF + 64;
#define STAMP(i)
#define STAMPV(i, v)
#define STAMP_FLUSH()
static_assert(LDS_BYTES <= 160 * 1024, "LDS");
constexpr int C_LANDED = 0  , C_WQ = 2, C_TICKET = 3, C_DONE = 4  , C_QRDY = 12, C_PRO = 13, C_SCALE = 14, C_ABORT = 15, C_ML = 16, C_WIN = 17, C_CKN = 18,
              C_EPF = 19, C_EP = 20  , C_CKT = 32  ;
constexpr int RED_OFF = 0;
constexpr int EX_OFF = 16384;

constexpr size_t WS_EPOCH = 0;
constexpr size_t WS_READY = 4096;
constexpr size_t WS_QG = 65536;
constexpr size_t WS_TABG = WS_QG + (size_t)B * HQ * DH * 8;
constexpr size_t WS_PROG = WS_TABG + 65536;
constexpr size_t WS_OB = WS_PROG + 32768;
constexpr int OB_BYTES = 4096, OB_O = 0, OB_P = 2048, OB_ML = 3840;
constexpr int WS_END = (int)(WS_OB + (size_t)NWG * OB_BYTES);
constexpr int OOB = 0x7ffffff0;
constexpr unsigned SENT = 0xFFFFFFFFu;
constexpr long long TCAP = 5000000;

__device__ __forceinline__ float bf_lo(unsigned u) { return __builtin_bit_cast(float, u << 16); }
__device__ __forceinline__ float bf_hi(unsigned u) { return __builtin_bit_cast(float, u & 0xffff0000u); }
__device__ __forceinline__ float bf1(u16 u) { return __builtin_bit_cast(float, (unsigned)u << 16); }
__device__ __forceinline__ float rbf(float x) { return (float)(__bf16)x; }
__device__ __forceinline__ unsigned bfbits(float x) { return (unsigned)__builtin_bit_cast(u16, (__bf16)x); }
__device__ __forceinline__ float dot2(unsigned a, unsigned b, float acc) {
  return __builtin_amdgcn_fdot2_f32_bf16(__builtin_bit_cast(bf16x2, a), __builtin_bit_cast(bf16x2, b), acc, false);
}
template <int CTRL> __device__ __forceinline__ float dpp(float x) {
  return __builtin_bit_cast(float, __builtin_amdgcn_mov_dpp(__builtin_bit_cast(int, x), CTRL, 0xf, 0xf, true));
}
constexpr int XOR1 = 0xB1, XOR2 = 0x4E, XOR7 = 0x141, ROR8 = 0x128, ROR4 = 0x124;
__device__ __forceinline__ float swap16_add(float a, float b) {
  auto r = __builtin_amdgcn_permlane16_swap(__float_as_uint(a), __float_as_uint(b), false, false);
  return __uint_as_float(r[0]) + __uint_as_float(r[1]);
}
__device__ __forceinline__ float swap32_add(float a, float b) {
  auto r = __builtin_amdgcn_permlane32_swap(__float_as_uint(a), __float_as_uint(b), false, false);
  return __uint_as_float(r[0]) + __uint_as_float(r[1]);
}
__device__ __forceinline__ float swap16_max(float a) {
  auto r = __builtin_amdgcn_permlane16_swap(__float_as_uint(a), __float_as_uint(a), false, false);
  return fmaxf(__uint_as_float(r[0]), __uint_as_float(r[1]));
}
__device__ __forceinline__ float swap32_max(float a) {
  auto r = __builtin_amdgcn_permlane32_swap(__float_as_uint(a), __float_as_uint(a), false, false);
  return fmaxf(__uint_as_float(r[0]), __uint_as_float(r[1]));
}
__device__ __forceinline__ float wave_sum(float x) {
  x += dpp<XOR1>(x); x += dpp<XOR2>(x); x += dpp<XOR7>(x); x += dpp<ROR8>(x);
  x = swap16_add(x, x);
  return swap32_add(x, x);
}
__device__ __forceinline__ float first_lane(float v) { return __builtin_bit_cast(float, __builtin_amdgcn_readfirstlane(__builtin_bit_cast(int, v))); }
__device__ __forceinline__ float fdiv(float a, float b, float rb) {
  const float q = a * rb;
  return fmaf(fmaf(-q, b, a), rb, q);
}
__device__ __forceinline__ float ln_apply(float x, float mean, float var) {
  const float m = rbf(mean);
  const float den = rbf(__builtin_amdgcn_sqrtf(rbf(rbf(var) + EPS)));
  return rbf(fdiv(rbf(x - m), den, __builtin_amdgcn_rcpf(den)));
}
__device__ __forceinline__ void ln448(float (&v)[7]) {
  const float c = first_lane(v[0]);
  float s1 = 0.f, s2 = 0.f;
#pragma unroll
  for (int i = 0; i < 7; ++i) { const float d = v[i] - c; s1 += d; s2 = fmaf(d, d, s2); }
  s1 = wave_sum(s1) * (1.f / D);
  s2 = wave_sum(s2) * (1.f / D);
  const float var = fmaxf(fmaf(-s1, s1, s2), 0.f), mean = c + s1;
#pragma unroll
  for (int i = 0; i < 7; ++i) v[i] = ln_apply(v[i], mean, var);
}
__device__ __forceinline__ float mixer_unit(float t, float w1, float b1) {
  constexpr float K0 = -2.f * 0.7978845608028654f * 1.4426950408889634f, K1 = K0 * 0.044715f;
  const float u = rbf(rbf(t * w1) + b1);
  const float z = u * fmaf(u * u, K1, K0);
  return rbf(u * __builtin_amdgcn_rcpf(1.f + __builtin_amdgcn_exp2f(z)));
}
__device__ __forceinline__ u64 rt() { return __builtin_amdgcn_s_memrealtime(); }
#define LDS_WAIT() asm volatile("s_waitcnt lgkmcnt(0)" ::: "memory")
#define DRAIN() asm volatile("s_waitcnt vmcnt(0)" ::: "memory")
#define BARRIER() asm volatile("s_waitcnt lgkmcnt(0)\n\ts_barrier" ::: "memory")
#define GLDS(src, dst) __builtin_amdgcn_global_load_lds((const GAS void*)(src), (LAS void*)(dst), 16, 0, 2)
__device__ __forceinline__ unsigned nosent(float v) { const unsigned u = __float_as_uint(v); return u == SENT ? 0x7FC00000u : u; }
__device__ __forceinline__ bool full4(u32x4 v) { return max(max(v[0], v[1]), max(v[2], v[3])) != SENT; }

#define POLL32(r, off, rs) asm volatile("buffer_load_dword %0, %1, %2, 0 offen sc1" : "=v"(r) : "v"(off), "s"(rs) : "memory")
#define POLL128(r, off, rs) asm volatile("buffer_load_dwordx4 %0, %1, %2, 0 offen sc1" : "=v"(r) : "v"(off), "s"(rs) : "memory")

struct Args {
  const u16* x; const u16* kc; const u16* vc; const u16* wq; const u16* wo;
  const u16* w1; const u16* b1; const u16* w2; const u16* b2;
  u16* out; unsigned char* ws;
};

__global__ void __launch_bounds__(NTHR, 3) k_fused(Args a) {
  extern __shared__ __attribute__((aligned(16))) unsigned char smem[];
  LAS unsigned char* L8 = (LAS unsigned char*)smem;
  volatile LAS unsigned* ctl = (volatile LAS unsigned*)(L8 + CTL_OFF);
  const int tid = threadIdx.x, lane = tid & 63, wave = __builtin_amdgcn_readfirstlane(tid >> 6);
  const int bid = blockIdx.x, kvh = bid & 7, b = (bid >> 3) & 7, s = bid >> 6;
  const bool is_final = (kvh == b) && (s == 0);
  GAS unsigned char* ws = (GAS unsigned char*)a.ws;
  const size_t chunk = ((size_t)(b * HKV + kvh) * SK + (size_t)s * (SK / 4)) * (DH * 2);
  const auto rs_ws = __builtin_amdgcn_make_buffer_rsrc((void*)a.ws, 0, WS_END, 0x00020000);
  auto expired = [&](u64 t0, unsigned& sp) -> bool {
    if ((++sp & 255u) != 0u) return false;
    if (ctl[C_ABORT] != 0u) return true;
    if ((long long)(rt() - t0) > TCAP) { ctl[C_ABORT] = 1u; return true; }
    return false;
  };
  auto wait_ge = [&](int word, unsigned need, int nap) {
    if (ctl[word] < need) {
      const u64 t0 = rt(); unsigned sp = 0;
      do { if (nap == 1) __builtin_amdgcn_s_sleep(1); else if (nap > 1) __builtin_amdgcn_s_sleep(8); if (expired(t0, sp)) break; } while (ctl[word] < need);
    }
    asm volatile("" ::: "memory");
  };
  auto uniform64 = [](u64 v) -> u64 {
    return (u64)(unsigned)__builtin_amdgcn_readfirstlane((int)(unsigned)v) | ((u64)(unsigned)__builtin_amdgcn_readfirstlane((int)(unsigned)(v >> 32)) << 32);
  };
  constexpr float C2 = 0.0625f * 1.4426950408889634f;
  auto get_epoch1 = [&]() -> u64 {
    wait_ge(C_EPF, 1u, 0);
    return ((u64)ctl[C_EP] | ((u64)ctl[C_EP + 1] << 32)) + 1ull;
  };
  constexpr u64 TAGM = 0xFFFFFFFFFFFFull;

  if (__builtin_expect(wave < NL, 1)) {
    const int L = wave;
    __builtin_amdgcn_s_setprio(3);
    const GAS unsigned char* ksrc = (const GAS unsigned char*)a.kc + chunk + lane * 16;
    const int rot = CFG_ROT ? ((bid >> 6) & 1) * 16 + ((bid >> 5) & 1) * 32 : 0;
#pragma unroll 1
    for (int j = 0; j < 8 * PRE; ++j) {
      const int o = ((j >> 3) * NL + L) * TILE_BYTES + (j & 7) * 1024;
      GLDS(ksrc + rot * TILE_BYTES + o, L8 + RING_OFF + o);
    }
    BARRIER();
    const GAS unsigned char* kbase = (const GAS unsigned char*)a.kc + chunk;
    const GAS unsigned char* vbase = (const GAS unsigned char*)a.vc + chunk;
    const unsigned vo = lane * 16;
    auto draw = [&]() -> unsigned {
      unsigned t; u64 keep;
      asm volatile("s_mov_b64 %1, exec\n\ts_mov_b64 exec, 1\n\tds_add_rtn_u32 %0, %2, %3\n\ts_mov_b64 exec, %1"
                   : "=&v"(t), "=&s"(keep) : "v"((unsigned)(size_t)(L8 + CTL_OFF + C_TICKET * 4)), "v"(1u) : "memory");
      return t;
    };
    bool dead = false;
    int pub = 0, h1 = (PRE - 1) * NL + L, h2 = PRE > 1 ? (PRE - 2) * NL + L : 0;
    unsigned cv = 0, nxt = draw();
    for (;;) {
      asm volatile("s_waitcnt lgkmcnt(0)" : "+v"(nxt)::"memory");
      const int T = PRE * NL + __builtin_amdgcn_readfirstlane((int)nxt);
      if (T >= NTILES) break;
      if (T >= NS) {
        const unsigned need = (unsigned)((T - NS) / NC + 1);
        if ((unsigned)__builtin_amdgcn_readlane((int)cv, C_DONE + (T & (NC - 1))) < need && ctl[C_DONE + (T & (NC - 1))] < need) {
          DRAIN();
          pub = T;
          ctl[C_LANDED + L] = (unsigned)pub;
          const u64 t0 = rt(); unsigned sp = 0;
          while (ctl[C_DONE + (T & (NC - 1))] < need) { __builtin_amdgcn_s_sleep(1); if (expired(t0, sp)) { dead = true; break; } }
          if (dead) break;
        }
        asm volatile("" ::: "memory");
      }
      const int wnow = __builtin_amdgcn_readlane((int)cv, C_WIN);
      LAS unsigned char* dst = L8 + RING_OFF + (T & (NS - 1)) * TILE_BYTES;
      const GAS unsigned char* ub = (T < TPP ? kbase : vbase) + ((T + rot) & (TPP - 1)) * TILE_BYTES;
#define PIECE(p) __builtin_amdgcn_global_load_lds((const GAS void*)(ub + ((p) >> 2) * 4096 + vo), (LAS void*)(dst + ((p) >> 2) * 4096), 16, ((p) & 3) * 1024, 2)
#define STEP(Wn, j)                                                                                     \
        PIECE(j); PIECE(j + 1);                                                                         \
        if (j == 6) { nxt = draw(); cv = ctl[lane & 31]; }                                              \
        asm volatile("s_waitcnt vmcnt(%0)" ::"i"(Wn) : "memory");                                       \
        if ((j + 34 - (Wn)) % 8 < 2) {                   \
          constexpr int back = 4 - (j + 34 - (Wn)) / 8;     \
          const int v = back <= 0 ? T : back == 1 ? h1 : h2;                                            \
          if (v > pub) { pub = v; ctl[C_LANDED + L] = (unsigned)pub; }                                  \
        }
#define TILE_BODY(Wn) { STEP(Wn, 0) STEP(Wn, 2) STEP(Wn, 4) STEP(Wn, 6) }
      switch (wnow) {
        case 1: TILE_BODY(WMIN) break;
        case 2: TILE_BODY(WMIN + 1) break;
        case 3: TILE_BODY(WMIN + 2) break;
        case 5: TILE_BODY(WMIN + 4) break;
        case 6: TILE_BODY(WMIN + 5) break;
        case 7: TILE_BODY(WMIN + 6) break;
        case 8: TILE_BODY(WMIN + 7) break;
        default: TILE_BODY(WIN) break;
      }
      h2 = h1; h1 = T;
      if (T == TPP) STAMP(7);
      if (T == 32) STAMP(29);
      if (T == 64) STAMP(30);
      if (T == 192) STAMP(31);
      if ((T % CKSTEP) == 0) { ctl[C_CKT + T / CKSTEP - 1] = (unsigned)rt(); ctl[C_CKN] = (unsigned)(T / CKSTEP); }
    }
    if (!dead) {
      asm volatile("s_waitcnt vmcnt(16)" ::: "memory");
      if (h2 > pub) ctl[C_LANDED + L] = (unsigned)h2;
      asm volatile("s_waitcnt vmcnt(8)" ::: "memory");
      if (h1 > pub) ctl[C_LANDED + L] = (unsigned)h1;
      DRAIN();
      ctl[C_LANDED + L] = (unsigned)NTILES;
      if (h1 == NTILES - 1) STAMP(8);
    } else {
      DRAIN();
    }
    LDS_WAIT();
    return;
  }

  if (wave == PACER) {
    BARRIER();
    const u64 ep1 = uniform64(get_epoch1()), tag = ep1 & TAGM;
#pragma unroll 1
    for (int c = 1; c <= NCK; ++c) {
      if (c == TPP / CKSTEP) {
        const u64 t0 = rt(); unsigned sp = 0;
        const int pk = is_final ? (lane & 7) : kvh, ps = (lane >> 3) & 3;
        const GAS u64* rf = (const GAS u64*)(ws + WS_READY) + (pk + 8 * b + 64 * ps);
        for (;;) {
          const u64 v = __hip_atomic_load(rf, RLX_AGENT);
          if (__all(v == ep1)) break;
          __builtin_amdgcn_s_sleep(16);
          sp += 15u;
          if (expired(t0, sp)) break;
        }
      }
      wait_ge(C_CKN, (unsigned)c, c == TPP / CKSTEP ? 1 : 2);
      const unsigned tc = ctl[C_CKT + c - 1];
      GAS u64* pg = (GAS u64*)(ws + WS_PROG) + (c - 1) * NWG;
      if (lane == 0) __hip_atomic_store(pg + bid, (tag << 16) | (u64)(tc & 0xffffu), RLX_AGENT);
      if (c == TPP / CKSTEP) {
        const u64 t0 = rt(); unsigned sp = 0;
        wait_ge(C_ML, (unsigned)NC, 1);
        const LAS float* ml = (const LAS float*)(L8 + ML_OFF) + (lane & 15) * 2;
        const float mw = ml[0], lw = ml[1];
        float M = fmaxf(mw, dpp<XOR2>(mw)); M = fmaxf(M, dpp<ROR4>(M)); M = fmaxf(M, dpp<ROR8>(M));
        float Ls = lw * __builtin_amdgcn_exp2f((mw - M) * C2);
        Ls += dpp<XOR2>(Ls); Ls += dpp<ROR4>(Ls); Ls += dpp<ROR8>(Ls);
        if (lane < 2) __builtin_amdgcn_raw_buffer_store_b64(u32x2{nosent(M), nosent(Ls)}, rs_ws, (int)WS_OB + bid * OB_BYTES + OB_ML + lane * 8, 0, 16);
        const int off = (int)WS_OB + (kvh + 8 * b + 64 * (lane >> 4)) * OB_BYTES + OB_ML + (lane & 1) * 8;
        u32x2 v = {SENT, SENT};
        __builtin_amdgcn_s_sleep(CFG_MLDELAY);
        for (;;) {
          __builtin_amdgcn_s_sleep(8);
          if (v[0] == SENT || v[1] == SENT) v = __builtin_amdgcn_raw_buffer_load_b64(rs_ws, off, 0, 16);
          if (__all(v[0] != SENT && v[1] != SENT)) break;
          sp += 15u;
          if (expired(t0, sp)) break;
        }
        const float Ms = __uint_as_float(v[0]);
        const float Mg = swap32_max(swap16_max(Ms));
        float Lg = __uint_as_float(v[1]) * __builtin_amdgcn_exp2f((Ms - Mg) * C2);
        Lg = swap16_add(Lg, Lg); Lg = swap32_add(Lg, Lg);
        if (lane < 2) { LAS float* gs = (LAS float*)(L8 + GS_OFF) + lane * 2; gs[0] = Mg; gs[1] = 1.f / Lg; }
        LDS_WAIT();
        ctl[C_SCALE] = 1u;
        STAMP(24);
      }
      while ((unsigned)rt() - tc < (unsigned)CFG_PDELAY) __builtin_amdgcn_s_sleep(16);
      const u64 g = __hip_atomic_load(pg + (bid & ~63) + lane, RLX_AGENT);
      const int d = (int)(short)(u16)(tc - (unsigned)g);
      const float df = ((g >> 16) == tag) ? (float)min(max(d, -(CFG_PDELAY - 100)), 600) : -(float)(CFG_PDELAY - 100);
      const float lag = wave_sum(df) * (1.f / 64.f);
      const int w = min(max(WIN + (int)rintf(lag * CFG_PGAIN), WMIN), WMAX);
      ctl[C_WIN] = (unsigned)(w - WMIN + 1);
    }
    LDS_WAIT();
    return;
  }

  if (wave == SVC) {
    u64 epoch = __hip_atomic_load((GAS u64*)(ws + WS_EPOCH), RLX_AGENT);
    ((LAS unsigned*)(L8 + CTL_OFF))[lane] = 0u;
    BARRIER();
    asm volatile("" : "+v"(epoch));
    epoch = uniform64(epoch);
    ctl[C_EP] = (unsigned)epoch; ctl[C_EP + 1] = (unsigned)(epoch >> 32);
    LDS_WAIT();
    ctl[C_EPF] = 1u;
    const u64 tag = (epoch + 1ull) & TAGM;
    {
      const GAS u64* qg = (const GAS u64*)(ws + WS_QG) + b * (HQ * DH) + kvh * 512 + lane;
      u64 g[8];
#pragma unroll
      for (int i = 0; i < 8; ++i) g[i] = ~tag << 16;
      wait_ge(C_PRO, (unsigned)NC, 1);
      const u64 t0 = rt(); unsigned sp = 0;
      while ((long long)(rt() - t0) < CFG_QDELAY) __builtin_amdgcn_s_sleep(4);
      for (;;) {
        bool ok = true;
#pragma unroll
        for (int i = 0; i < 8; ++i) if ((g[i] >> 16) != tag) g[i] = __hip_atomic_load(qg + i * 64, RLX_AGENT);
#pragma unroll
        for (int i = 0; i < 8; ++i) ok &= ((g[i] >> 16) == tag);
        if (__all(ok)) break;
        __builtin_amdgcn_s_sleep(CFG_QSLEEP);
        sp += 15u;
        if (expired(t0, sp)) break;
      }
#pragma unroll
      for (int i = 0; i < 8; ++i) ((LAS u16*)(L8 + Q_OFF))[i * 64 + lane] = (u16)g[i];
      LDS_WAIT();
      ctl[C_QRDY] = 1u;
      STAMP(4);
    }
#pragma unroll
    for (int i = 0; i < 4; ++i)
      __builtin_amdgcn_raw_buffer_store_b128(u32x4{SENT, SENT, SENT, SENT}, rs_ws, (int)WS_OB + bid * OB_BYTES + i * 1024 + lane * 16, 0, 16);
    unsigned pw1[14], pb1[14], pw2[14];
#pragma unroll
    for (int k = 0; k < 14; ++k) {
      pw1[k] = __builtin_nontemporal_load((const GAS unsigned*)a.w1 + lane + 64 * k);
      pb1[k] = __builtin_nontemporal_load((const GAS unsigned*)a.b1 + lane + 64 * k);
      pw2[k] = __builtin_nontemporal_load((const GAS unsigned*)a.w2 + lane + 64 * k);
    }
    const float b2 = bf1(__builtin_nontemporal_load((const GAS u16*)a.b2));
    DRAIN();
    if (lane == 0) __hip_atomic_store((GAS u64*)(ws + WS_READY) + bid, epoch + 1ull, RLX_AGENT);
    {
      unsigned mine = 0;
#pragma unroll 1
      for (int e = 0; e < NE; ++e) {
        const float t = bf1((u16)(((unsigned)(bid >> 7) << 15) | ((unsigned)(E0 + e) << 7) | (unsigned)(bid & 127)));
        float acc0 = 0.f, acc1 = 0.f;
#pragma unroll
        for (int k = 0; k < 14; ++k) {
          acc0 = fmaf(mixer_unit(t, bf_lo(pw1[k]), bf_lo(pb1[k])), bf_lo(pw2[k]), acc0);
          acc1 = fmaf(mixer_unit(t, bf_hi(pw1[k]), bf_hi(pb1[k])), bf_hi(pw2[k]), acc1);
        }
        const float y = rbf(rbf(wave_sum(acc0 + acc1)) + b2);
        if (lane == e) mine = bfbits(y);
      }
      if (lane < NE) __hip_atomic_store((GAS u64*)(ws + WS_TABG) + bid * NE + lane, (tag << 16) | (u64)mine, RLX_AGENT);
    }
    STAMP(15);
    if (is_final) {
      const GAS u64* tg = (const GAS u64*)(ws + WS_TABG) + lane;
      const u64 t0 = rt(); unsigned sp = 0;
      while ((long long)(rt() - t0) < 400) __builtin_amdgcn_s_sleep(32);
#pragma unroll 1
      for (int c = 0; c < 256 * NE / 512; ++c) {
        for (;;) {
          u64 g[8]; bool ok = true;
#pragma unroll
          for (int i = 0; i < 8; ++i) g[i] = __hip_atomic_load(tg + c * 512 + i * 64, RLX_AGENT);
#pragma unroll
          for (int i = 0; i < 8; ++i) { ok &= ((g[i] >> 16) == tag); ((LAS u16*)(L8 + TAB_OFF))[c * 512 + i * 64 + lane] = (u16)g[i]; }
          if (__all(ok)) break;
          __builtin_amdgcn_s_sleep(32);
          sp += 15u;
          if (expired(t0, sp)) break;
        }
      }
    }
    STAMP(16);
    LDS_WAIT();
    return;
  }

  const int cw = wave - NL, ct = tid - NL * 64;
  const int par = lane >> 5, c = lane & 31;
  {
    unsigned xr[7];
#pragma unroll
    for (int i = 0; i < 7; ++i) xr[i] = __builtin_nontemporal_load((const GAS u16*)a.x + cw * D + lane + 64 * i);
    if (cw < 7) {
      const GAS unsigned char* wsrc = (const GAS unsigned char*)a.wq + (size_t)(cw * 64 + (lane >> 1)) * (HQ * DH * 2) + (kvh * 512 + (bid >> 3) * 16) * 2 + (lane & 1) * 16;
      __builtin_amdgcn_global_load_lds((const GAS void*)wsrc, (LAS void*)(L8 + PB_OFF + cw * 2048), 16, 0, WQ_AUX);
      __builtin_amdgcn_global_load_lds((const GAS void*)(wsrc + (size_t)32 * (HQ * DH * 2)), (LAS void*)(L8 + PB_OFF + cw * 2048 + 1024), 16, 0, WQ_AUX);
    }
    BARRIER();
    DRAIN();
    if (lane == 0) __hip_atomic_fetch_add((LAS unsigned*)(L8 + CTL_OFF) + C_WQ, 1u, __ATOMIC_RELAXED, __HIP_MEMORY_SCOPE_WORKGROUP);
    asm volatile("" : "+v"(xr[0]), "+v"(xr[1]), "+v"(xr[2]), "+v"(xr[3]), "+v"(xr[4]), "+v"(xr[5]), "+v"(xr[6]));
    float xn[7];
#pragma unroll
    for (int i = 0; i < 7; ++i) xn[i] = bf_lo(xr[i]);
    ln448(xn);
    wait_ge(C_WQ, (unsigned)NC, 0);
    if (cw == 0) STAMP(2);
    float acc[16];
#pragma unroll
    for (int k = 0; k < 16; ++k) acc[k] = 0.f;
#pragma unroll
    for (int i = 0; i < 7; ++i) {
      const LAS u32x4* wp = (const LAS u32x4*)(L8 + PB_OFF + (lane + 64 * i) * 32);
      const u32x4 w0 = wp[0], w1 = wp[1];
      const unsigned xhi = __float_as_uint(xn[i]), xlo = xhi >> 16;
#pragma unroll
      for (int k = 0; k < 4; ++k) {
        acc[2 * k] = dot2(w0[k], xlo, acc[2 * k]); acc[2 * k + 1] = dot2(w0[k], xhi, acc[2 * k + 1]);
        acc[8 + 2 * k] = dot2(w1[k], xlo, acc[8 + 2 * k]); acc[8 + 2 * k + 1] = dot2(w1[k], xhi, acc[8 + 2 * k + 1]);
      }
    }
    LDS_WAIT();
    if (lane == 0) __hip_atomic_fetch_add((LAS unsigned*)(L8 + CTL_OFF) + C_PRO, 1u, __ATOMIC_RELAXED, __HIP_MEMORY_SCOPE_WORKGROUP);
#pragma unroll
    for (int j = 0; j < 8; ++j) acc[j] = swap32_add(acc[j], acc[j + 8]);
#pragma unroll
    for (int j = 0; j < 4; ++j) acc[j] = swap16_add(acc[j], acc[j + 4]);
#pragma unroll
    for (int j = 0; j < 2; ++j) { const float t1 = acc[j] + dpp<ROR8>(acc[j]), t2 = acc[j + 2] + dpp<ROR8>(acc[j + 2]); acc[j] = (lane & 8) ? t2 : t1; }
    { const float t1 = acc[0] + dpp<XOR7>(acc[0]), t2 = acc[1] + dpp<XOR7>(acc[1]); acc[0] = (lane & 4) ? t2 : t1; }
    float qv = acc[0] + dpp<XOR2>(acc[0]);
    qv += dpp<XOR1>(qv);
    const u64 tag = get_epoch1() & TAGM;
    if ((lane & 3) == 0)
      __hip_atomic_store((GAS u64*)(ws + WS_QG) + cw * (HQ * DH) + kvh * 512 + (bid >> 3) * 16 + (lane >> 2), (tag << 16) | (u64)bfbits(qv), RLX_AGENT);
    if (cw == 0) STAMP(3);
  }
  LAS float* pb = (LAS float*)(L8 + PB_OFF) + cw * 512;
  auto wait_tile = [&](int T, bool nap) {
    const volatile LAS u64* lp = (const volatile LAS u64*)(L8 + CTL_OFF + C_LANDED * 4);
    u64 v = *lp;
    if ((unsigned)v <= (unsigned)T || (unsigned)(v >> 32) <= (unsigned)T) {
      const u64 t0 = rt(); unsigned sp = 0;
      do { if (nap) __builtin_amdgcn_s_sleep(1); v = *lp; if (expired(t0, sp)) break; } while ((unsigned)v <= (unsigned)T || (unsigned)(v >> 32) <= (unsigned)T);
    }
    asm volatile("" ::: "memory");
  };
  auto fetch = [&](u32x4 (&R)[8], int T, unsigned n) {
    wait_tile(T, true);
    const LAS u32x4* tp = (const LAS u32x4*)(L8 + RING_OFF + (T & (NS - 1)) * TILE_BYTES) + lane;
#pragma unroll
    for (int i = 0; i < 8; ++i) R[i] = tp[i * 64];
    LDS_WAIT();
    ctl[C_DONE + cw] = n;
    asm volatile("" : "+v"(R[0]), "+v"(R[1]), "+v"(R[2]), "+v"(R[3]), "+v"(R[4]), "+v"(R[5]), "+v"(R[6]), "+v"(R[7])::"memory");
  };
  const int hs = (lane >> 1) & 1;
  const int spos = par * 16 + hs * 8 + ((lane >> 2) & 7);
  f32x16 acc;
#pragma unroll
  for (int k = 0; k < 16; ++k) acc[k] = 0.f;
  unsigned am[4];
  {
    const int j0 = lane & 7;
#pragma unroll
    for (int d = 0; d < 4; ++d) am[d] = (c < 16 && (j0 >> 1) == d) ? ((j0 & 1) ? 0xffff0000u : 0x0000ffffu) : 0u;
  }
  auto pv = [&](const u32x4 (&V)[8], int j) {
    const LAS u32x4* pp = (const LAS u32x4*)(pb + j * 32 + par * 16 + ((lane >> 3) & 1) * 8);
    const u32x4 p0 = pp[0], p1 = pp[1];
#pragma unroll
    for (int i = 0; i < 8; ++i) {
      const unsigned pd = i < 4 ? p0[i & 3] : p1[i & 3];
      const u32x4 A = {pd & am[0], pd & am[1], pd & am[2], pd & am[3]};
      acc = __builtin_amdgcn_mfma_f32_32x32x16_bf16(__builtin_bit_cast(bf16x8, A), __builtin_bit_cast(bf16x8, V[i]), acc, 0, 0, 0);
    }
  };
  {
    u32x4 Ra[8], Rb[8];
    fetch(Ra, cw, 1u);
    fetch(Rb, cw + NC, 2u);
    wait_ge(C_QRDY, 1u, 1);
    const u32x4 q0 = *(const LAS u32x4*)(L8 + Q_OFF + c * 16), q1 = *(const LAS u32x4*)(L8 + Q_OFF + 512 + c * 16);
    float mx = -INFINITY;
    auto score = [&](const u32x4 (&K)[8], int j) {
      float x[16];
#pragma unroll
      for (int i = 0; i < 8; ++i) {
        float d0 = 0.f, d1 = 0.f;
#pragma unroll
        for (int k = 0; k < 4; ++k) { d0 = dot2(K[i][k], q0[k], d0); d1 = dot2(K[i][k], q1[k], d1); }
        x[2 * i] = d0; x[2 * i + 1] = d1;
      }
#pragma unroll
      for (int u = 0; u < 8; ++u) x[u] = swap16_add(x[u], x[u + 8]);
#pragma unroll
      for (int u = 0; u < 4; ++u) { const float t1 = x[u] + dpp<ROR8>(x[u]), t2 = x[u + 4] + dpp<ROR8>(x[u + 4]); x[u] = (lane & 8) ? t2 : t1; }
#pragma unroll
      for (int u = 0; u < 2; ++u) { const float t1 = x[u] + dpp<XOR7>(x[u]), t2 = x[u + 2] + dpp<XOR7>(x[u + 2]); x[u] = (lane & 4) ? t2 : t1; }
      { const float t1 = x[0] + dpp<XOR2>(x[0]), t2 = x[1] + dpp<XOR2>(x[1]); x[0] = (lane & 2) ? t2 : t1; }
      const float sc = rbf(x[0] + dpp<XOR1>(x[0]));
      mx = fmaxf(mx, sc);
      pb[j * 32 + spos] = sc;
    };
#pragma unroll 1
    for (int j = 0; j < TPP / NC; j += 2) {
      score(Ra, j);
      if (cw == 0 && j == 0) STAMP(5);
      if (j + 2 < TPP / NC) fetch(Ra, cw + NC * (j + 2), (unsigned)(j + 3));
      score(Rb, j + 1);
      if (j + 2 < TPP / NC) fetch(Rb, cw + NC * (j + 3), (unsigned)(j + 4));
    }
    if (cw == 0) STAMP(6);
    mx = fmaxf(mx, dpp<ROR4>(mx)); mx = fmaxf(mx, dpp<ROR8>(mx));
    mx = swap32_max(swap16_max(mx));
    LDS_WAIT();
    float sv[8], ls = 0.f;
#pragma unroll
    for (int j2 = 0; j2 < 8; ++j2) {
      sv[j2] = pb[(j2 * 2 + (lane & 1)) * 32 + spos];
      ls += __builtin_amdgcn_exp2f((sv[j2] - mx) * C2);
    }
    ls += dpp<XOR1>(ls); ls += dpp<ROR4>(ls); ls += dpp<ROR8>(ls);
    ls = swap16_add(ls, ls);
    ls = swap32_add(ls, ls);
    if (lane == 0 || lane == 2) { LAS float* ml = (LAS float*)(L8 + ML_OFF) + (cw * 2 + hs) * 2; ml[0] = mx; ml[1] = ls; }
    LDS_WAIT();
    if (lane == 0) __hip_atomic_fetch_add((LAS unsigned*)(L8 + CTL_OFF) + C_ML, 1u, __ATOMIC_RELAXED, __HIP_MEMORY_SCOPE_WORKGROUP);
    fetch(Ra, TPP + cw, (unsigned)(TPP / NC + 1));
    fetch(Rb, TPP + cw + NC, (unsigned)(TPP / NC + 2));
    wait_ge(C_SCALE, 1u, 1);
    {
      const LAS float* gs = (const LAS float*)(L8 + GS_OFF) + hs * 2;
      const float Mg = gs[0], inv = gs[1];
#pragma unroll
      for (int j2 = 0; j2 < 8; ++j2) {
        const unsigned pbits = bfbits(__builtin_amdgcn_exp2f((sv[j2] - Mg) * C2) * inv);
        pb[(j2 * 2 + (lane & 1)) * 32 + spos] = __uint_as_float(pbits | (pbits << 16));
        if (j2 == 7 && cw >= NC - 2 && (lane & 1)) ((LAS unsigned*)(L8 + Q_OFF))[(cw - (NC - 2)) * 32 + spos] = pbits | (pbits << 16);
      }
      LDS_WAIT();
    }
    pv(Ra, 0);
    pv(Rb, 1);
  }
  u32x4 W[16];
  const auto rs_wo = __builtin_amdgcn_make_buffer_rsrc((void*)(a.wo + (size_t)((2 * kvh + (s >> 1)) * DH + (s & 1) * 128 + 16 * cw) * D), 0, 16 * D * 2, 0x00020000);
  const int wvo = (lane < 56 ? lane : 55) * 16;
  const int fcol = cw * 64 + (lane & 15) * 4 + (lane >> 4);
  unsigned xres = 0;
  if (is_final && cw < 7) xres = __builtin_nontemporal_load((const GAS u16*)a.x + b * D + fcol);
#pragma unroll 1
  for (int j = 2; j < TPP / NC - 1; ++j) {
    if (j >= CFG_WJ0 && j < CFG_WJ0 + 8) {
#pragma unroll
      for (int i = 0; i < 8; ++i) if (j == i + CFG_WJ0) {
        W[2 * i] = __builtin_amdgcn_raw_buffer_load_b128(rs_wo, wvo, (2 * i) * (D * 2), WO_AUX);
        W[2 * i + 1] = __builtin_amdgcn_raw_buffer_load_b128(rs_wo, wvo, (2 * i + 1) * (D * 2), WO_AUX);
      }
    }
    u32x4 V[8];
    fetch(V, TPP + cw + NC * j, (unsigned)(TPP / NC + j + 1));
    pv(V, j);
  }
  {
#pragma unroll
    for (int i = 0; i < 8; ++i)
#pragma unroll
      for (int k = 0; k < 4; ++k) {
        const unsigned w0 = W[2 * i][k], w1 = W[2 * i + 1][k];
        W[2 * i][k] = __builtin_amdgcn_perm(w1, w0, 0x05040100u);
        W[2 * i + 1][k] = __builtin_amdgcn_perm(w1, w0, 0x07060302u);
      }
    asm volatile("" : "+v"(W[0]), "+v"(W[1]), "+v"(W[2]), "+v"(W[3]), "+v"(W[4]), "+v"(W[5]), "+v"(W[6]), "+v"(W[7]), "+v"(xres));
    asm volatile("" : "+v"(W[8]), "+v"(W[9]), "+v"(W[10]), "+v"(W[11]), "+v"(W[12]), "+v"(W[13]), "+v"(W[14]), "+v"(W[15]));
    wait_ge(C_DONE + NC - 2, (unsigned)(TPP / NC + 3), 1);
    wait_ge(C_DONE + NC - 1, (unsigned)(TPP / NC + 3), 1);
    const LAS unsigned* sp2 = (const LAS unsigned*)(L8 + Q_OFF) + par * 16 + ((lane >> 3) & 1) * 8 + cw;
    const unsigned pd0 = sp2[0], pd1 = sp2[32];
    const u32x4 A0 = {pd0 & am[0], pd0 & am[1], pd0 & am[2], pd0 & am[3]}, A1 = {pd1 & am[0], pd1 & am[1], pd1 & am[2], pd1 & am[3]};
    if (cw < NC - 2) {
      u32x4 V[8];
      fetch(V, NTILES - NC + cw, (unsigned)(NTILES / NC));
      pv(V, TPP / NC - 1);
    }
    wait_tile(NTILES - 2, true);
    const u32x4 V0 = *(const LAS u32x4*)(L8 + RING_OFF + ((NTILES - 2) & (NS - 1)) * TILE_BYTES + cw * 1024 + lane * 16);
    acc = __builtin_amdgcn_mfma_f32_32x32x16_bf16(__builtin_bit_cast(bf16x8, A0), __builtin_bit_cast(bf16x8, V0), acc, 0, 0, 0);
    wait_tile(NTILES - 1, false);
    const u32x4 V1 = *(const LAS u32x4*)(L8 + RING_OFF + ((NTILES - 1) & (NS - 1)) * TILE_BYTES + cw * 1024 + lane * 16);
    acc = __builtin_amdgcn_mfma_f32_32x32x16_bf16(__builtin_bit_cast(bf16x8, A1), __builtin_bit_cast(bf16x8, V1), acc, 0, 0, 0);
    LAS float* rec = pb + c * 8 + par * 4;
    *(LAS f32x4*)rec = f32x4{acc[0], acc[1], acc[2], acc[3]};
    *(LAS f32x4*)(rec + 256) = f32x4{acc[4], acc[5], acc[6], acc[7]};
  }
  if (cw == 7) STAMP(9);
  BARRIER();
  if (cw == 0) STAMP(10);
  if (ct < 128) {
    f32x4 o = *(const LAS f32x4*)((const LAS float*)(L8 + PB_OFF) + ct * 4);
#pragma unroll
    for (int w = 1; w < NC; ++w) o += *(const LAS f32x4*)((const LAS float*)(L8 + PB_OFF) + w * 512 + ct * 4);
    __builtin_amdgcn_raw_buffer_store_b128(u32x4{nosent(o[0]), nosent(o[1]), nosent(o[2]), nosent(o[3])}, rs_ws, (int)WS_OB + bid * OB_BYTES + OB_O + ct * 16, 0, 16);
  }
  if (cw == 0) STAMP(25);
  const u32x4 rsv = {(unsigned)(size_t)a.ws, (unsigned)((size_t)a.ws >> 32) & 0xffffu, (unsigned)WS_END, 0x00020000u};
  {
    const int off = (int)WS_OB + (kvh + 8 * b + 64 * (lane >> 4)) * OB_BYTES + OB_O + (s * 128 + cw * 16 + (lane & 15)) * 4;
    unsigned ov = SENT;
    {
      const u64 t0 = rt(); unsigned sp = 0;
      if (CFG_H1D0) __builtin_amdgcn_s_sleep(CFG_H1D0);
      for (;;) {
        if (ov == SENT) ov = __builtin_amdgcn_raw_buffer_load_b32(rs_ws, off, 0, 16);
        if (__all(ov != SENT)) break;
        if (CFG_H1D1) __builtin_amdgcn_s_sleep(CFG_H1D1);
        sp += 15u;
        if (expired(t0, sp)) break;
      }
    }
    if (cw == 0) STAMP(11);
    float ot = __uint_as_float(ov);
    ot = swap16_add(ot, ot); ot = swap32_add(ot, ot);
    const float ob = rbf(ot);
    const unsigned opair = (__float_as_uint(ob) >> 16) | __float_as_uint(dpp<XOR1>(ob));
    float pa[8];
#pragma unroll
    for (int k = 0; k < 8; ++k) pa[k] = 0.f;
#pragma unroll
    for (int i = 0; i < 8; ++i) {
      const unsigned op = (unsigned)__builtin_amdgcn_readlane((int)opair, 2 * i);
#pragma unroll
      for (int k = 0; k < 4; ++k) { pa[2 * k] = dot2(W[2 * i][k], op, pa[2 * k]); pa[2 * k + 1] = dot2(W[2 * i + 1][k], op, pa[2 * k + 1]); }
    }
    if (lane < 56) {
      LAS float* red = (LAS float*)(L8 + RED_OFF) + cw * D + lane * 8;
      *(LAS f32x4*)red = f32x4{pa[0], pa[1], pa[2], pa[3]};
      *(LAS f32x4*)(red + 4) = f32x4{pa[4], pa[5], pa[6], pa[7]};
    }
  }
  if (cw == 0) STAMP(12);
  BARRIER();
  if (cw == 0) STAMP(26);
  if (ct < 112) {
    f32x4 v = *(const LAS f32x4*)((const LAS float*)(L8 + RED_OFF) + ct * 4);
#pragma unroll
    for (int w = 1; w < NC; ++w) v += *(const LAS f32x4*)((const LAS float*)(L8 + RED_OFF) + w * D + ct * 4);
    __builtin_amdgcn_raw_buffer_store_b128(u32x4{nosent(v[0]), nosent(v[1]), nosent(v[2]), nosent(v[3])}, rs_ws, (int)WS_OB + bid * OB_BYTES + OB_P + ct * 16, 0, 16);
  }
  if (cw == 0) STAMP(27);
  if (!is_final) { if (cw == 0) STAMP_FLUSH(); return; }
  if (cw == 7) return;
  {
    const int off = (int)WS_OB + (8 * b + 64 * (lane >> 4)) * OB_BYTES + OB_P + (cw * 64 + (lane & 15) * 4) * 4;
    u32x4 v[8];
#pragma unroll
    for (int i = 0; i < 8; ++i) v[i] = u32x4{SENT, SENT, SENT, SENT};
    {
      const u64 t0 = rt(); unsigned sp = 0;
      if (CFG_H2D0) __builtin_amdgcn_s_sleep(CFG_H2D0);
      for (;;) {
        bool ok = true;
#pragma unroll
        for (int i = 0; i < 8; ++i) if (!full4(v[i])) v[i] = __builtin_amdgcn_raw_buffer_load_b128(rs_ws, off + i * OB_BYTES, 0, 16);
#pragma unroll
        for (int i = 0; i < 8; ++i) ok &= full4(v[i]);
        if (__all(ok)) break;
        sp += 15u;
        if (expired(t0, sp)) break;
      }
    }
    if (cw == 0) STAMP(13);
    f32x4 sum = __builtin_bit_cast(f32x4, v[0]);
#pragma unroll
    for (int i = 1; i < 8; ++i) sum += __builtin_bit_cast(f32x4, v[i]);
    const float e0 = swap32_add(sum[0], sum[2]), e1 = swap32_add(sum[1], sum[3]);
    const float att = swap16_add(e0, e1);
    const float h = rbf(bf_lo(xres) + rbf(att));
    float t = h;
#pragma unroll
    for (int r = 0; r < 2; ++r) {
      const float cs = first_lane(t), d = t - cs;
      const float s1 = wave_sum(d), s2 = wave_sum(d * d);
      LAS float* ex = (LAS float*)(L8 + EX_OFF) + r * 16;
      if (lane == 0) { ex[cw * 2] = fmaf(s1, 1.f / 64.f, cs); ex[cw * 2 + 1] = fmaxf(fmaf(-s1 * (1.f / 64.f), s1, s2), 0.f); }
      BARRIER();
      if (cw == 0) STAMP(22 + r);
      const f32x4 x0 = *(const LAS f32x4*)ex, x1 = *(const LAS f32x4*)(ex + 4), x2 = *(const LAS f32x4*)(ex + 8);
      const float m6 = ex[12], q6 = ex[13];
      const float mean = (((x0[0] + x0[2]) + (x1[0] + x1[2])) + ((x2[0] + x2[2]) + m6)) * (1.f / 7.f);
      float dv = 0.f;
      { const float d0 = x0[0] - mean, d1 = x0[2] - mean, d2 = x1[0] - mean, d3 = x1[2] - mean, d4 = x2[0] - mean, d5 = x2[2] - mean, d6 = m6 - mean;
        dv = ((d0 * d0 + d1 * d1) + (d2 * d2 + d3 * d3)) + ((d4 * d4 + d5 * d5) + d6 * d6); }
      const float m2 = (((x0[1] + x0[3]) + (x1[1] + x1[3])) + ((x2[1] + x2[3]) + q6)) + 64.f * dv;
      t = ln_apply(t, mean, m2 * (1.f / D));
    }
    const unsigned bits = __float_as_uint(t) >> 16;
    const unsigned e = ((bits >> 7) & 0xffu) - (unsigned)E0;
    const bool miss = e >= (unsigned)NE;
    float y = bf1(((const LAS u16*)(L8 + TAB_OFF))[(((bits >> 15) << 7) | (bits & 127u)) * NE + (miss ? 0u : e)]);
    u64 mm = __ballot(miss);
    while (mm) {
      const int src = __builtin_ctzll(mm); mm &= mm - 1;
      const float tv = __builtin_bit_cast(float, __builtin_amdgcn_readlane(__builtin_bit_cast(int, t), src));
      float ya = 0.f;
#pragma unroll 2
      for (int f = lane; f < F; f += 64) ya = fmaf(mixer_unit(tv, bf1(((const GAS u16*)a.w1)[f]), bf1(((const GAS u16*)a.b1)[f])), bf1(((const GAS u16*)a.w2)[f]), ya);
      const float yv = rbf(rbf(wave_sum(ya)) + bf1(*(const GAS u16*)a.b2));
      if (lane == src) y = yv;
    }
    ((GAS u16*)a.out)[b * D + fcol] = (u16)bfbits(h + y);
    if (b == 0 && ct == 0) __hip_atomic_store((GAS u64*)(ws + WS_EPOCH), ((u64)ctl[C_EP] | ((u64)ctl[C_EP + 1] << 32)) + 1ull, RLX_AGENT);
    if (cw == 0) { STAMP(14); STAMP_FLUSH(); }
  }
}
}

extern "C" void kernel_launch(void* const* d_in, const int* in_sizes, int n_in, void* d_out, int out_size, void* d_ws, size_t ws_size,
                              hipStream_t stream) {
  Args a;
  a.x = (const u16*)d_in[0]; a.kc = (const u16*)d_in[1]; a.vc = (const u16*)d_in[2]; a.wq = (const u16*)d_in[3]; a.wo = (const u16*)d_in[4];
  a.w1 = (const u16*)d_in[6]; a.b1 = (const u16*)d_in[7]; a.w2 = (const u16*)d_in[8]; a.b2 = (const u16*)d_in[9];
  a.out = (u16*)d_out; a.ws = (unsigned char*)d_ws;
  (void)hipFuncSetAttribute((const void*)k_fused, hipFuncAttributeMaxDynamicSharedMemorySize, LDS_BYTES);
  k_fused<<<NWG, NTHR, LDS_BYTES, stream>>>(a);
}
```

```cpp
#include <hip/hip_runtime.h>
#include <hip/hip_bf16.h>
#include <cstdint>

namespace {
#define GAS __attribute__((address_space(1)))
#define LAS __attribute__((address_space(3)))
#define RLX_AGENT __ATOMIC_RELAXED, __HIP_MEMORY_SCOPE_AGENT
typedef unsigned u32x4 __attribute__((ext_vector_type(4)));
typedef unsigned u32x2 __attribute__((ext_vector_type(2)));
typedef float f32x4 __attribute__((ext_vector_type(4)));
typedef float f32x16 __attribute__((ext_vector_type(16)));
typedef short bf16x8 __attribute__((ext_vector_type(8)));
typedef __bf16 bf16x2 __attribute__((ext_vector_type(2)));
typedef unsigned long long u64;
typedef unsigned short u16;

#define CFG_GRP 2
#define CFG_CKSTEP 32
#define CFG_PGAIN 0.030f
#define CFG_PDELAY 250
#define CFG_WIN 12
#define CFG_QDELAY 220
#define CFG_QSLEEP 32
#define WO_AUX 2
#define CFG_PRE 1
#define WQ_AUX 2
#define CFG_H1D0 10
#define CFG_H1D1 0
#define CFG_H2D0 0
#define CFG_PERM 1
#define CFG_PGRP 0
#define CFG_SHARE 1
#define CFG_H1PIPE 0
#define CFG_TLB 0
#define CFG_ROT 1
#define CFG_MLDELAY 32
#define CFG_WJ0 6
#define CFG_H1STAG 4
#define CFG_H2PIPE 0
#define CFG_H2STAG 6
constexpr int B = 8, D = 448, HQ = 16, HKV = 8, SK = 8192, DH = 256, F = 1792;
constexpr int NWG = 256, NL = 2, NC = 8, NWAVES = NL + NC + 2, NTHR = NWAVES * 64, SVC = NL + NC, PACER = SVC + 1;
constexpr int TILE_BYTES = 8192, NS = 16;
constexpr int TPP = 128, NTILES = 2 * TPP;
constexpr int GRP = CFG_GRP;
constexpr int WIN = CFG_WIN;
constexpr int PRE = CFG_PRE;
constexpr int WMIN = WIN - 3, WMAX = WIN + 4;
constexpr int CKSTEP = CFG_CKSTEP, NCK = (NTILES - 1) / CKSTEP;
static_assert(NL == 2 && WMIN >= 4 && WMAX <= 16 + GRP && NCK <= 16 && CKSTEP % NL == 0 && 8 % GRP == 0 && TPP % CKSTEP == 0, "window");
constexpr float EPS = 1e-5f;
constexpr int NE = 24, E0 = 108;

constexpr int RING_OFF = 0;
constexpr int PB_OFF = NS * TILE_BYTES;
constexpr int TAB_OFF = PB_OFF + 16384;
constexpr int Q_OFF = TAB_OFF + 256 * NE * 2;
constexpr int CTL_OFF = Q_OFF + 1024;
constexpr int ML_OFF = CTL_OFF + 256, GS_OFF = ML_OFF + 128;
constexpr int LDS_BYTES = GS_OFF + 64;
#define STAMP(i)
#define STAMPV(i, v)
#define STAMP_FLUSH()
static_assert(LDS_BYTES <= 160 * 1024, "LDS");
constexpr int C_LANDED = 0  , C_WQ = 2, C_TICKET = 3, C_DONE = 4  , C_QRDY = 12, C_PRO = 13, C_SCALE = 14, C_ABORT = 15, C_ML = 16, C_WIN = 17, C_CKN = 18,
              C_EPF = 19, C_EP = 20  , C_CKT = 32  ;
constexpr int RED_OFF = 0;
constexpr int EX_OFF = 16384;

constexpr size_t WS_EPOCH = 0;
constexpr size_t WS_READY = 4096;
constexpr size_t WS_QG = 65536;
constexpr size_t WS_TABG = WS_QG + (size_t)B * HQ * DH * 8;
constexpr size_t WS_PROG = WS_TABG + 65536;
constexpr size_t WS_OB = WS_PROG + 32768;
constexpr int OB_BYTES = 4096, OB_O = 0, OB_P = 2048, OB_ML = 3840;
constexpr int WS_END = (int)(WS_OB + (size_t)NWG * OB_BYTES);
constexpr int OOB = 0x7ffffff0;
constexpr unsigned SENT = 0xFFFFFFFFu;
constexpr long long TCAP = 5000000;

__device__ __forceinline__ float bf_lo(unsigned u) { return __builtin_bit_cast(float, u << 16); }
__device__ __forceinline__ float bf_hi(unsigned u) { return __builtin_bit_cast(float, u & 0xffff0000u); }
__device__ __forceinline__ float bf1(u16 u) { return __builtin_bit_cast(float, (unsigned)u << 16); }
__device__ __forceinline__ float rbf(float x) { return (float)(__bf16)x; }
__device__ __forceinline__ unsigned bfbits(float x) { return (unsigned)__builtin_bit_cast(u16, (__bf16)x); }
__device__ __forceinline__ float dot2(unsigned a, unsigned b, float acc) {
  return __builtin_amdgcn_fdot2_f32_bf16(__builtin_bit_cast(bf16x2, a), __builtin_bit_cast(bf16x2, b), acc, false);
}
template <int CTRL> __device__ __forceinline__ float dpp(float x) {
  return __builtin_bit_cast(float, __builtin_amdgcn_mov_dpp(__builtin_bit_cast(int, x), CTRL, 0xf, 0xf, true));
}
constexpr int XOR1 = 0xB1, XOR2 = 0x4E, XOR7 = 0x141, ROR8 = 0x128, ROR4 = 0x124;
__device__ __forceinline__ float swap16_add(float a, float b) {
  auto r = __builtin_amdgcn_permlane16_swap(__float_as_uint(a), __float_as_uint(b), false, false);
  return __uint_as_float(r[0]) + __uint_as_float(r[1]);
}
__device__ __forceinline__ float swap32_add(float a, float b) {
  auto r = __builtin_amdgcn_permlane32_swap(__float_as_uint(a), __float_as_uint(b), false, false);
  return __uint_as_float(r[0]) + __uint_as_float(r[1]);
}
__device__ __forceinline__ float swap16_max(float a) {
  auto r = __builtin_amdgcn_permlane16_swap(__float_as_uint(a), __float_as_uint(a), false, false);
  return fmaxf(__uint_as_float(r[0]), __uint_as_float(r[1]));
}
__device__ __forceinline__ float swap32_max(float a) {
  auto r = __builtin_amdgcn_permlane32_swap(__float_as_uint(a), __float_as_uint(a), false, false);
  return fmaxf(__uint_as_float(r[0]), __uint_as_float(r[1]));
}
__device__ __forceinline__ float wave_sum(float x) {
  x += dpp<XOR1>(x); x += dpp<XOR2>(x); x += dpp<XOR7>(x); x += dpp<ROR8>(x);
  x = swap16_add(x, x);
  return swap32_add(x, x);
}
__device__ __forceinline__ float first_lane(float v) { return __builtin_bit_cast(float, __builtin_amdgcn_readfirstlane(__builtin_bit_cast(int, v))); }
__device__ __forceinline__ float fdiv(float a, float b, float rb) {
  const float q = a * rb;
  return fmaf(fmaf(-q, b, a), rb, q);
}
__device__ __forceinline__ float ln_apply(float x, float mean, float var) {
  const float m = rbf(mean);
  const float den = rbf(__builtin_amdgcn_sqrtf(rbf(rbf(var) + EPS)));
  return rbf(fdiv(rbf(x - m), den, __builtin_amdgcn_rcpf(den)));
}
__device__ __forceinline__ void ln448(float (&v)[7]) {
  const float c = first_lane(v[0]);
  float s1 = 0.f, s2 = 0.f;
#pragma unroll
  for (int i = 0; i < 7; ++i) { const float d = v[i] - c; s1 += d; s2 = fmaf(d, d, s2); }
  s1 = wave_sum(s1) * (1.f / D);
  s2 = wave_sum(s2) * (1.f / D);
  const float var = fmaxf(fmaf(-s1, s1, s2), 0.f), mean = c + s1;
#pragma unroll
  for (int i = 0; i < 7; ++i) v[i] = ln_apply(v[i], mean, var);
}
__device__ __forceinline__ float mixer_unit(float t, float w1, float b1) {
  constexpr float K0 = -2.f * 0.7978845608028654f * 1.4426950408889634f, K1 = K0 * 0.044715f;
  const float u = rbf(rbf(t * w1) + b1);
  const float z = u * fmaf(u * u, K1, K0);
  return rbf(u * __builtin_amdgcn_rcpf(1.f + __builtin_amdgcn_exp2f(z)));
}
__device__ __forceinline__ u64 rt() { return __builtin_amdgcn_s_memrealtime(); }
#define LDS_WAIT() asm volatile("s_waitcnt lgkmcnt(0)" ::: "memory")
#define DRAIN() asm volatile("s_waitcnt vmcnt(0)" ::: "memory")
#define BARRIER() asm volatile("s_waitcnt lgkmcnt(0)\n\ts_barrier" ::: "memory")
#define GLDS(src, dst) __builtin_amdgcn_global_load_lds((const GAS void*)(src), (LAS void*)(dst), 16, 0, 2)
__device__ __forceinline__ unsigned nosent(float v) { const unsigned u = __float_as_uint(v); return u == SENT ? 0x7FC00000u : u; }
__device__ __forceinline__ bool full4(u32x4 v) { return max(max(v[0], v[1]), max(v[2], v[3])) != SENT; }

#define POLL32(r, off, rs) asm volatile("buffer_load_dword %0, %1, %2, 0 offen sc1" : "=v"(r) : "v"(off), "s"(rs) : "memory")
#define POLL128(r, off, rs) asm volatile("buffer_load_dwordx4 %0, %1, %2, 0 offen sc1" : "=v"(r) : "v"(off), "s"(rs) : "memory")

struct Args {
  const u16* x; const u16* kc; const u16* vc; const u16* wq; const u16* wo;
  const u16* w1; const u16* b1; const u16* w2; const u16* b2;
  u16* out; unsigned char* ws;
};

__global__ void __launch_bounds__(NTHR, 3) k_fused(Args a) {
  extern __shared__ __attribute__((aligned(16))) unsigned char smem[];
  LAS unsigned char* L8 = (LAS unsigned char*)smem;
  volatile LAS unsigned* ctl = (volatile LAS unsigned*)(L8 + CTL_OFF);
  const int tid = threadIdx.x, lane = tid & 63, wave = __builtin_amdgcn_readfirstlane(tid >> 6);
  const int bid = blockIdx.x, kvh = bid & 7, b = (bid >> 3) & 7, s = bid >> 6;
  const bool is_final = (kvh == b) && (s == 0);
  GAS unsigned char* ws = (GAS unsigned char*)a.ws;
  const size_t chunk = ((size_t)(b * HKV + kvh) * SK + (size_t)s * (SK / 4)) * (DH * 2);
  const auto rs_ws = __builtin_amdgcn_make_buffer_rsrc((void*)a.ws, 0, WS_END, 0x00020000);
  auto expired = [&](u64 t0, unsigned& sp) -> bool {
    if ((++sp & 255u) != 0u) return false;
    if (ctl[C_ABORT] != 0u) return true;
    if ((long long)(rt() - t0) > TCAP) { ctl[C_ABORT] = 1u; return true; }
    return false;
  };
  auto wait_ge = [&](int word, unsigned need, int nap) {
    if (ctl[word] < need) {
      const u64 t0 = rt(); unsigned sp = 0;
      do { if (nap == 1) __builtin_amdgcn_s_sleep(1); else if (nap > 1) __builtin_amdgcn_s_sleep(8); if (expired(t0, sp)) break; } while (ctl[word] < need);
    }
    asm volatile("" ::: "memory");
  };
  auto uniform64 = [](u64 v) -> u64 {
    return (u64)(unsigned)__builtin_amdgcn_readfirstlane((int)(unsigned)v) | ((u64)(unsigned)__builtin_amdgcn_readfirstlane((int)(unsigned)(v >> 32)) << 32);
  };
  constexpr float C2 = 0.0625f * 1.4426950408889634f;
  auto get_epoch1 = [&]() -> u64 {
    wait_ge(C_EPF, 1u, 0);
    return ((u64)ctl[C_EP] | ((u64)ctl[C_EP + 1] << 32)) + 1ull;
  };
  constexpr u64 TAGM = 0xFFFFFFFFFFFFull;

  if (__builtin_expect(wave < NL, 1)) {
    const int L = wave;
    __builtin_amdgcn_s_setprio(3);
    const GAS unsigned char* ksrc = (const GAS unsigned char*)a.kc + chunk + lane * 16;
    const int rot = CFG_ROT == 0 ? 0 : CFG_ROT == 1 ? ((bid >> 6) & 1) * 16 + ((bid >> 5) & 1) * 32
                  : CFG_ROT == 2 ? (bid & 127) : CFG_ROT == 3 ? (int)(((unsigned)(bid + 1) * 2654435761u >> 13) * 2246822519u >> 25)
                  : CFG_ROT == 4 ? (int)(((unsigned)(bid + 77) * 3266489917u >> 11) * 668265263u >> 25)
                  : CFG_ROT == 5 ? ((bid >> 6) & 1) * 16 + ((bid >> 5) & 1) * 32 + (bid & 1) * 64 + ((bid >> 1) & 1) * 8 + ((bid >> 2) & 1) * 4
                  : (s * 16 + ((bid >> 5) & 1) * 64);
#define TILE_POS(T) (((((T) >> CFG_PGRP) * CFG_PERM << CFG_PGRP) + ((T) & ((1 << CFG_PGRP) - 1)) + rot) & (TPP - 1))
#pragma unroll 1
    for (int j = 0; j < 8 * PRE; ++j) {
      const int t = (j >> 3) * NL + L;
      GLDS(ksrc + TILE_POS(t) * TILE_BYTES + (j & 7) * 1024, L8 + RING_OFF + t * TILE_BYTES + (j & 7) * 1024);
    }
    BARRIER();
    const GAS unsigned char* kbase = (const GAS unsigned char*)a.kc + chunk;
    const GAS unsigned char* vbase = (const GAS unsigned char*)a.vc + chunk;
    const unsigned vo = lane * 16;
    auto draw = [&]() -> unsigned {
      unsigned t; u64 keep;
      asm volatile("s_mov_b64 %1, exec\n\ts_mov_b64 exec, 1\n\tds_add_rtn_u32 %0, %2, %3\n\ts_mov_b64 exec, %1"
                   : "=&v"(t), "=&s"(keep) : "v"((unsigned)(size_t)(L8 + CTL_OFF + C_TICKET * 4)), "v"(1u) : "memory");
      return t;
    };
    bool dead = false;
    int pub = 0, h1 = (PRE - 1) * NL + L, h2 = PRE > 1 ? (PRE - 2) * NL + L : 0;
    unsigned cv = 0, nxt = draw();
    for (;;) {
      asm volatile("s_waitcnt lgkmcnt(0)" : "+v"(nxt)::"memory");
      const int T = PRE * NL + __builtin_amdgcn_readfirstlane((int)nxt);
      if (T >= NTILES) break;
      if (T >= NS) {
        const unsigned need = (unsigned)((T - NS) / NC + 1);
        if ((unsigned)__builtin_amdgcn_readlane((int)cv, C_DONE + (T & (NC - 1))) < need && ctl[C_DONE + (T & (NC - 1))] < need) {
          DRAIN();
          pub = T;
          ctl[C_LANDED + L] = (unsigned)pub;
          const u64 t0 = rt(); unsigned sp = 0;
          while (ctl[C_DONE + (T & (NC - 1))] < need) { __builtin_amdgcn_s_sleep(1); if (expired(t0, sp)) { dead = true; break; } }
          if (dead) break;
        }
        asm volatile("" ::: "memory");
      }
      const int wnow = __builtin_amdgcn_readlane((int)cv, C_WIN);
      LAS unsigned char* dst = L8 + RING_OFF + (T & (NS - 1)) * TILE_BYTES;
      const GAS unsigned char* ub = (T < TPP ? kbase : vbase) + TILE_POS(T) * TILE_BYTES;
#define PIECE(p) __builtin_amdgcn_global_load_lds((const GAS void*)(ub + ((p) >> 2) * 4096 + vo), (LAS void*)(dst + ((p) >> 2) * 4096), 16, ((p) & 3) * 1024, 2)
#define STEP(Wn, j)                                                                                     \
        PIECE(j); PIECE(j + 1);                                                                         \
        if (j == 6) { nxt = draw(); cv = ctl[lane & 31]; }                                              \
        asm volatile("s_waitcnt vmcnt(%0)" ::"i"(Wn) : "memory");                                       \
        if ((j + 34 - (Wn)) % 8 < 2) {                   \
          constexpr int back = 4 - (j + 34 - (Wn)) / 8;     \
          const int v = back <= 0 ? T : back == 1 ? h1 : h2;                                            \
          if (v > pub) { pub = v; ctl[C_LANDED + L] = (unsigned)pub; }                                  \
        }
#define TILE_BODY(Wn) { STEP(Wn, 0) STEP(Wn, 2) STEP(Wn, 4) STEP(Wn, 6) }
      switch (wnow) {
        case 1: TILE_BODY(WMIN) break;
        case 2: TILE_BODY(WMIN + 1) break;
        case 3: TILE_BODY(WMIN + 2) break;
        case 5: TILE_BODY(WMIN + 4) break;
        case 6: TILE_BODY(WMIN + 5) break;
        case 7: TILE_BODY(WMIN + 6) break;
        case 8: TILE_BODY(WMIN + 7) break;
        default: TILE_BODY(WIN) break;
      }
      h2 = h1; h1 = T;
      if (T == TPP) STAMP(7);
      if (T == 32) STAMP(29);
      if (T == 64) STAMP(30);
      if (T == 192) STAMP(31);
      if ((T % CKSTEP) == 0) { ctl[C_CKT + T / CKSTEP - 1] = (unsigned)rt(); ctl[C_CKN] = (unsigned)(T / CKSTEP); }
    }
    if (!dead) {
      asm volatile("s_waitcnt vmcnt(16)" ::: "memory");
      if (h2 > pub) ctl[C_LANDED + L] = (unsigned)h2;
      asm volatile("s_waitcnt vmcnt(8)" ::: "memory");
      if (h1 > pub) ctl[C_LANDED + L] = (unsigned)h1;
      DRAIN();
      ctl[C_LANDED + L] = (unsigned)NTILES;
      if (h1 == NTILES - 1) STAMP(8);
    } else {
      DRAIN();
    }
    LDS_WAIT();
    return;
  }

  if (wave == PACER) {
    BARRIER();
    const u64 ep1 = uniform64(get_epoch1()), tag = ep1 & TAGM;
#pragma unroll 1
    for (int c = 1; c <= NCK; ++c) {
      if (c == TPP / CKSTEP) {
        const u64 t0 = rt(); unsigned sp = 0;
        const int pk = is_final ? (lane & 7) : kvh, ps = (lane >> 3) & 3;
        const GAS u64* rf = (const GAS u64*)(ws + WS_READY) + (pk + 8 * b + 64 * ps);
        for (;;) {
          const u64 v = __hip_atomic_load(rf, RLX_AGENT);
          if (__all(v == ep1)) break;
          __builtin_amdgcn_s_sleep(16);
          sp += 15u;
          if (expired(t0, sp)) break;
        }
      }
      wait_ge(C_CKN, (unsigned)c, c == TPP / CKSTEP ? 1 : 2);
      const unsigned tc = ctl[C_CKT + c - 1];
      GAS u64* pg = (GAS u64*)(ws + WS_PROG) + (c - 1) * NWG;
      if (lane == 0) __hip_atomic_store(pg + bid, (tag << 16) | (u64)(tc & 0xffffu), RLX_AGENT);
      if (c == TPP / CKSTEP) {
        const u64 t0 = rt(); unsigned sp = 0;
        wait_ge(C_ML, (unsigned)NC, 1);
        const LAS float* ml = (const LAS float*)(L8 + ML_OFF) + (lane & 15) * 2;
        const float mw = ml[0], lw = ml[1];
        float M = fmaxf(mw, dpp<XOR2>(mw)); M = fmaxf(M, dpp<ROR4>(M)); M = fmaxf(M, dpp<ROR8>(M));
        float Ls = lw * __builtin_amdgcn_exp2f((mw - M) * C2);
        Ls += dpp<XOR2>(Ls); Ls += dpp<ROR4>(Ls); Ls += dpp<ROR8>(Ls);
        if (lane < 2) __builtin_amdgcn_raw_buffer_store_b64(u32x2{nosent(M), nosent(Ls)}, rs_ws, (int)WS_OB + bid * OB_BYTES + OB_ML + lane * 8, 0, 16);
        const int off = (int)WS_OB + (kvh + 8 * b + 64 * (lane >> 4)) * OB_BYTES + OB_ML + (lane & 1) * 8;
        u32x2 v = {SENT, SENT};
        __builtin_amdgcn_s_sleep(CFG_MLDELAY);
        for (;;) {
          __builtin_amdgcn_s_sleep(8);
          if (v[0] == SENT || v[1] == SENT) v = __builtin_amdgcn_raw_buffer_load_b64(rs_ws, off, 0, 16);
          if (__all(v[0] != SENT && v[1] != SENT)) break;
          sp += 15u;
          if (expired(t0, sp)) break;
        }
        const float Ms = __uint_as_float(v[0]);
        const float Mg = swap32_max(swap16_max(Ms));
        float Lg = __uint_as_float(v[1]) * __builtin_amdgcn_exp2f((Ms - Mg) * C2);
        Lg = swap16_add(Lg, Lg); Lg = swap32_add(Lg, Lg);
        if (lane < 2) { LAS float* gs = (LAS float*)(L8 + GS_OFF) + lane * 2; gs[0] = Mg; gs[1] = 1.f / Lg; }
        LDS_WAIT();
        ctl[C_SCALE] = 1u;
        STAMP(24);
      }
      while ((unsigned)rt() - tc < (unsigned)CFG_PDELAY) __builtin_amdgcn_s_sleep(16);
      const u64 g = __hip_atomic_load(pg + (bid & ~63) + lane, RLX_AGENT);
      const int d = (int)(short)(u16)(tc - (unsigned)g);
      const float df = ((g >> 16) == tag) ? (float)min(max(d, -(CFG_PDELAY - 100)), 600) : -(float)(CFG_PDELAY - 100);
      const float lag = wave_sum(df) * (1.f / 64.f);
      const int w = min(max(WIN + (int)rintf(lag * CFG_PGAIN), WMIN), WMAX);
      ctl[C_WIN] = (unsigned)(w - WMIN + 1);
    }
    LDS_WAIT();
    return;
  }

  if (wave == SVC) {
    u64 epoch = __hip_atomic_load((GAS u64*)(ws + WS_EPOCH), RLX_AGENT);
    ((LAS unsigned*)(L8 + CTL_OFF))[lane] = 0u;
    BARRIER();
    asm volatile("" : "+v"(epoch));
    epoch = uniform64(epoch);
    ctl[C_EP] = (unsigned)epoch; ctl[C_EP + 1] = (unsigned)(epoch >> 32);
    LDS_WAIT();
    ctl[C_EPF] = 1u;
    const u64 tag = (epoch + 1ull) & TAGM;
    {
      const GAS u64* qg = (const GAS u64*)(ws + WS_QG) + b * (HQ * DH) + kvh * 512 + lane;
      u64 g[8];
#pragma unroll
      for (int i = 0; i < 8; ++i) g[i] = ~tag << 16;
      wait_ge(C_PRO, (unsigned)NC, 1);
      const u64 t0 = rt(); unsigned sp = 0;
      while ((long long)(rt() - t0) < CFG_QDELAY) __builtin_amdgcn_s_sleep(4);
      for (;;) {
        bool ok = true;
#pragma unroll
        for (int i = 0; i < 8; ++i) if ((g[i] >> 16) != tag) g[i] = __hip_atomic_load(qg + i * 64, RLX_AGENT);
#pragma unroll
        for (int i = 0; i < 8; ++i) ok &= ((g[i] >> 16) == tag);
        if (__all(ok)) break;
        __builtin_amdgcn_s_sleep(CFG_QSLEEP);
        sp += 15u;
        if (expired(t0, sp)) break;
      }
#pragma unroll
      for (int i = 0; i < 8; ++i) ((LAS u16*)(L8 + Q_OFF))[i * 64 + lane] = (u16)g[i];
      LDS_WAIT();
      ctl[C_QRDY] = 1u;
      STAMP(4);
    }
#pragma unroll
    for (int i = 0; i < 4; ++i)
      __builtin_amdgcn_raw_buffer_store_b128(u32x4{SENT, SENT, SENT, SENT}, rs_ws, (int)WS_OB + bid * OB_BYTES + i * 1024 + lane * 16, 0, 16);
    unsigned pw1[14], pb1[14], pw2[14];
#pragma unroll
    for (int k = 0; k < 14; ++k) {
      pw1[k] = __builtin_nontemporal_load((const GAS unsigned*)a.w1 + lane + 64 * k);
      pb1[k] = __builtin_nontemporal_load((const GAS unsigned*)a.b1 + lane + 64 * k);
      pw2[k] = __builtin_nontemporal_load((const GAS unsigned*)a.w2 + lane + 64 * k);
    }
    const float b2 = bf1(__builtin_nontemporal_load((const GAS u16*)a.b2));
    DRAIN();
    if (lane == 0) __hip_atomic_store((GAS u64*)(ws + WS_READY) + bid, epoch + 1ull, RLX_AGENT);
    {
      unsigned mine = 0;
#pragma unroll 1
      for (int e = 0; e < NE; ++e) {
        const float t = bf1((u16)(((unsigned)(bid >> 7) << 15) | ((unsigned)(E0 + e) << 7) | (unsigned)(bid & 127)));
        float acc0 = 0.f, acc1 = 0.f;
#pragma unroll
        for (int k = 0; k < 14; ++k) {
          acc0 = fmaf(mixer_unit(t, bf_lo(pw1[k]), bf_lo(pb1[k])), bf_lo(pw2[k]), acc0);
          acc1 = fmaf(mixer_unit(t, bf_hi(pw1[k]), bf_hi(pb1[k])), bf_hi(pw2[k]), acc1);
        }
        const float y = rbf(rbf(wave_sum(acc0 + acc1)) + b2);
        if (lane == e) mine = bfbits(y);
      }
      if (lane < NE) __hip_atomic_store((GAS u64*)(ws + WS_TABG) + bid * NE + lane, (tag << 16) | (u64)mine, RLX_AGENT);
    }
    STAMP(15);
    if (is_final) {
      const GAS u64* tg = (const GAS u64*)(ws + WS_TABG) + lane;
      const u64 t0 = rt(); unsigned sp = 0;
      while ((long long)(rt() - t0) < 400) __builtin_amdgcn_s_sleep(32);
#pragma unroll 1
      for (int c = 0; c < 256 * NE / 512; ++c) {
        for (;;) {
          u64 g[8]; bool ok = true;
#pragma unroll
          for (int i = 0; i < 8; ++i) g[i] = __hip_atomic_load(tg + c * 512 + i * 64, RLX_AGENT);
#pragma unroll
          for (int i = 0; i < 8; ++i) { ok &= ((g[i] >> 16) == tag); ((LAS u16*)(L8 + TAB_OFF))[c * 512 + i * 64 + lane] = (u16)g[i]; }
          if (__all(ok)) break;
          __builtin_amdgcn_s_sleep(32);
          sp += 15u;
          if (expired(t0, sp)) break;
        }
      }
    }
    STAMP(16);
    LDS_WAIT();
    return;
  }

  const int cw = wave - NL, ct = tid - NL * 64;
  const int par = lane >> 5, c = lane & 31;
  {
    unsigned xr[7];
#pragma unroll
    for (int i = 0; i < 7; ++i) xr[i] = __builtin_nontemporal_load((const GAS u16*)a.x + cw * D + lane + 64 * i);
    if (cw < 7) {
      const GAS unsigned char* wsrc = (const GAS unsigned char*)a.wq + (size_t)(cw * 64 + (lane >> 1)) * (HQ * DH * 2) + (kvh * 512 + (bid >> 3) * 16) * 2 + (lane & 1) * 16;
      __builtin_amdgcn_global_load_lds((const GAS void*)wsrc, (LAS void*)(L8 + PB_OFF + cw * 2048), 16, 0, WQ_AUX);
      __builtin_amdgcn_global_load_lds((const GAS void*)(wsrc + (size_t)32 * (HQ * DH * 2)), (LAS void*)(L8 + PB_OFF + cw * 2048 + 1024), 16, 0, WQ_AUX);
    }
    BARRIER();
    DRAIN();
    if (lane == 0) __hip_atomic_fetch_add((LAS unsigned*)(L8 + CTL_OFF) + C_WQ, 1u, __ATOMIC_RELAXED, __HIP_MEMORY_SCOPE_WORKGROUP);
    asm volatile("" : "+v"(xr[0]), "+v"(xr[1]), "+v"(xr[2]), "+v"(xr[3]), "+v"(xr[4]), "+v"(xr[5]), "+v"(xr[6]));
    float xn[7];
#pragma unroll
    for (int i = 0; i < 7; ++i) xn[i] = bf_lo(xr[i]);
    ln448(xn);
    wait_ge(C_WQ, (unsigned)NC, 0);
    if (cw == 0) STAMP(2);
    float acc[16];
#pragma unroll
    for (int k = 0; k < 16; ++k) acc[k] = 0.f;
#pragma unroll
    for (int i = 0; i < 7; ++i) {
      const LAS u32x4* wp = (const LAS u32x4*)(L8 + PB_OFF + (lane + 64 * i) * 32);
      const u32x4 w0 = wp[0], w1 = wp[1];
      const unsigned xhi = __float_as_uint(xn[i]), xlo = xhi >> 16;
#pragma unroll
      for (int k = 0; k < 4; ++k) {
        acc[2 * k] = dot2(w0[k], xlo, acc[2 * k]); acc[2 * k + 1] = dot2(w0[k], xhi, acc[2 * k + 1]);
        acc[8 + 2 * k] = dot2(w1[k], xlo, acc[8 + 2 * k]); acc[8 + 2 * k + 1] = dot2(w1[k], xhi, acc[8 + 2 * k + 1]);
      }
    }
    LDS_WAIT();
    if (lane == 0) __hip_atomic_fetch_add((LAS unsigned*)(L8 + CTL_OFF) + C_PRO, 1u, __ATOMIC_RELAXED, __HIP_MEMORY_SCOPE_WORKGROUP);
#pragma unroll
    for (int j = 0; j < 8; ++j) acc[j] = swap32_add(acc[j], acc[j + 8]);
#pragma unroll
    for (int j = 0; j < 4; ++j) acc[j] = swap16_add(acc[j], acc[j + 4]);
#pragma unroll
    for (int j = 0; j < 2; ++j) { const float t1 = acc[j] + dpp<ROR8>(acc[j]), t2 = acc[j + 2] + dpp<ROR8>(acc[j + 2]); acc[j] = (lane & 8) ? t2 : t1; }
    { const float t1 = acc[0] + dpp<XOR7>(acc[0]), t2 = acc[1] + dpp<XOR7>(acc[1]); acc[0] = (lane & 4) ? t2 : t1; }
    float qv = acc[0] + dpp<XOR2>(acc[0]);
    qv += dpp<XOR1>(qv);
    const u64 tag = get_epoch1() & TAGM;
    if ((lane & 3) == 0)
      __hip_atomic_store((GAS u64*)(ws + WS_QG) + cw * (HQ * DH) + kvh * 512 + (bid >> 3) * 16 + (lane >> 2), (tag << 16) | (u64)bfbits(qv), RLX_AGENT);
    if (cw == 0) STAMP(3);
  }
  LAS float* pb = (LAS float*)(L8 + PB_OFF) + cw * 512;
  auto wait_tile = [&](int T, bool nap) {
    const volatile LAS u64* lp = (const volatile LAS u64*)(L8 + CTL_OFF + C_LANDED * 4);
    u64 v = *lp;
    if ((unsigned)v <= (unsigned)T || (unsigned)(v >> 32) <= (unsigned)T) {
      const u64 t0 = rt(); unsigned sp = 0;
      do { if (nap) __builtin_amdgcn_s_sleep(1); v = *lp; if (expired(t0, sp)) break; } while ((unsigned)v <= (unsigned)T || (unsigned)(v >> 32) <= (unsigned)T);
    }
    asm volatile("" ::: "memory");
  };
  auto fetch = [&](u32x4 (&R)[8], int T, unsigned n) {
    wait_tile(T, true);
    const LAS u32x4* tp = (const LAS u32x4*)(L8 + RING_OFF + (T & (NS - 1)) * TILE_BYTES) + lane;
#pragma unroll
    for (int i = 0; i < 8; ++i) R[i] = tp[i * 64];
    LDS_WAIT();
    ctl[C_DONE + cw] = n;
    asm volatile("" : "+v"(R[0]), "+v"(R[1]), "+v"(R[2]), "+v"(R[3]), "+v"(R[4]), "+v"(R[5]), "+v"(R[6]), "+v"(R[7])::"memory");
  };
  const int hs = (lane >> 1) & 1;
  const int spos = par * 16 + hs * 8 + ((lane >> 2) & 7);
  f32x16 acc;
#pragma unroll
  for (int k = 0; k < 16; ++k) acc[k] = 0.f;
  unsigned am[4];
  {
    const int j0 = lane & 7;
#pragma unroll
    for (int d = 0; d < 4; ++d) am[d] = (c < 16 && (j0 >> 1) == d) ? ((j0 & 1) ? 0xffff0000u : 0x0000ffffu) : 0u;
  }
  auto pv = [&](const u32x4 (&V)[8], int j) {
    const LAS u32x4* pp = (const LAS u32x4*)(pb + j * 32 + par * 16 + ((lane >> 3) & 1) * 8);
    const u32x4 p0 = pp[0], p1 = pp[1];
#pragma unroll
    for (int i = 0; i < 8; ++i) {
      const unsigned pd = i < 4 ? p0[i & 3] : p1[i & 3];
      const u32x4 A = {pd & am[0], pd & am[1], pd & am[2], pd & am[3]};
      acc = __builtin_amdgcn_mfma_f32_32x32x16_bf16(__builtin_bit_cast(bf16x8, A), __builtin_bit_cast(bf16x8, V[i]), acc, 0, 0, 0);
    }
  };
  {
    u32x4 Ra[8], Rb[8];
    fetch(Ra, cw, 1u);
    fetch(Rb, cw + NC, 2u);
    wait_ge(C_QRDY, 1u, 1);
    const u32x4 q0 = *(const LAS u32x4*)(L8 + Q_OFF + c * 16), q1 = *(const LAS u32x4*)(L8 + Q_OFF + 512 + c * 16);
    float mx = -INFINITY;
    auto score = [&](const u32x4 (&K)[8], int j) {
      float x[16];
#pragma unroll
      for (int i = 0; i < 8; ++i) {
        float d0 = 0.f, d1 = 0.f;
#pragma unroll
        for (int k = 0; k < 4; ++k) { d0 = dot2(K[i][k], q0[k], d0); d1 = dot2(K[i][k], q1[k], d1); }
        x[2 * i] = d0; x[2 * i + 1] = d1;
      }
#pragma unroll
      for (int u = 0; u < 8; ++u) x[u] = swap16_add(x[u], x[u + 8]);
#pragma unroll
      for (int u = 0; u < 4; ++u) { const float t1 = x[u] + dpp<ROR8>(x[u]), t2 = x[u + 4] + dpp<ROR8>(x[u + 4]); x[u] = (lane & 8) ? t2 : t1; }
#pragma unroll
      for (int u = 0; u < 2; ++u) { const float t1 = x[u] + dpp<XOR7>(x[u]), t2 = x[u + 2] + dpp<XOR7>(x[u + 2]); x[u] = (lane & 4) ? t2 : t1; }
      { const float t1 = x[0] + dpp<XOR2>(x[0]), t2 = x[1] + dpp<XOR2>(x[1]); x[0] = (lane & 2) ? t2 : t1; }
      const float sc = rbf(x[0] + dpp<XOR1>(x[0]));
      mx = fmaxf(mx, sc);
      pb[j * 32 + spos] = sc;
    };
#pragma unroll 1
    for (int j = 0; j < TPP / NC; j += 2) {
      score(Ra, j);
      if (cw == 0 && j == 0) STAMP(5);
      if (j + 2 < TPP / NC) fetch(Ra, cw + NC * (j + 2), (unsigned)(j + 3));
      score(Rb, j + 1);
      if (j + 2 < TPP / NC) fetch(Rb, cw + NC * (j + 3), (unsigned)(j + 4));
    }
    if (cw == 0) STAMP(6);
    mx = fmaxf(mx, dpp<ROR4>(mx)); mx = fmaxf(mx, dpp<ROR8>(mx));
    mx = swap32_max(swap16_max(mx));
    LDS_WAIT();
    float sv[8], ls = 0.f;
#pragma unroll
    for (int j2 = 0; j2 < 8; ++j2) {
      sv[j2] = pb[(j2 * 2 + (lane & 1)) * 32 + spos];
      ls += __builtin_amdgcn_exp2f((sv[j2] - mx) * C2);
    }
    ls += dpp<XOR1>(ls); ls += dpp<ROR4>(ls); ls += dpp<ROR8>(ls);
    ls = swap16_add(ls, ls);
    ls = swap32_add(ls, ls);
    if (lane == 0 || lane == 2) { LAS float* ml = (LAS float*)(L8 + ML_OFF) + (cw * 2 + hs) * 2; ml[0] = mx; ml[1] = ls; }
    LDS_WAIT();
    if (lane == 0) __hip_atomic_fetch_add((LAS unsigned*)(L8 + CTL_OFF) + C_ML, 1u, __ATOMIC_RELAXED, __HIP_MEMORY_SCOPE_WORKGROUP);
    fetch(Ra, TPP + cw, (unsigned)(TPP / NC + 1));
    fetch(Rb, TPP + cw + NC, (unsigned)(TPP / NC + 2));
    wait_ge(C_SCALE, 1u, 1);
    {
      const LAS float* gs = (const LAS float*)(L8 + GS_OFF) + hs * 2;
      const float Mg = gs[0], inv = gs[1];
#pragma unroll
      for (int j2 = 0; j2 < 8; ++j2) {
        const unsigned pbits = bfbits(__builtin_amdgcn_exp2f((sv[j2] - Mg) * C2) * inv);
        pb[(j2 * 2 + (lane & 1)) * 32 + spos] = __uint_as_float(pbits | (pbits << 16));
        if (j2 == 7 && cw >= NC - 2 && (lane & 1)) ((LAS unsigned*)(L8 + Q_OFF))[(cw - (NC - 2)) * 32 + spos] = pbits | (pbits << 16);
      }
      LDS_WAIT();
    }
    pv(Ra, 0);
    pv(Rb, 1);
  }
  u32x4 W[16];
  const auto rs_wo = __builtin_amdgcn_make_buffer_rsrc((void*)(a.wo + (size_t)((2 * kvh + (s >> 1)) * DH + (s & 1) * 128 + 16 * cw) * D), 0, 16 * D * 2, 0x00020000);
  const int wvo = (lane < 56 ? lane : 55) * 16;
  const int fcol = cw * 64 + (lane & 15) * 4 + (lane >> 4);
  unsigned xres = 0;
  if (is_final && cw < 7) xres = __builtin_nontemporal_load((const GAS u16*)a.x + b * D + fcol);
#pragma unroll 1
  for (int j = 2; j < TPP / NC - 1; ++j) {
    if (j >= CFG_WJ0 && j < CFG_WJ0 + 8) {
#pragma unroll
      for (int i = 0; i < 8; ++i) if (j == i + CFG_WJ0) {
        W[2 * i] = __builtin_amdgcn_raw_buffer_load_b128(rs_wo, wvo, (2 * i) * (D * 2), WO_AUX);
        W[2 * i + 1] = __builtin_amdgcn_raw_buffer_load_b128(rs_wo, wvo, (2 * i + 1) * (D * 2), WO_AUX);
      }
    }
    u32x4 V[8];
    fetch(V, TPP + cw + NC * j, (unsigned)(TPP / NC + j + 1));
    pv(V, j);
  }
  {
#pragma unroll
    for (int i = 0; i < 8; ++i)
#pragma unroll
      for (int k = 0; k < 4; ++k) {
        const unsigned w0 = W[2 * i][k], w1 = W[2 * i + 1][k];
        W[2 * i][k] = __builtin_amdgcn_perm(w1, w0, 0x05040100u);
        W[2 * i + 1][k] = __builtin_amdgcn_perm(w1, w0, 0x07060302u);
      }
    asm volatile("" : "+v"(W[0]), "+v"(W[1]), "+v"(W[2]), "+v"(W[3]), "+v"(W[4]), "+v"(W[5]), "+v"(W[6]), "+v"(W[7]), "+v"(xres));
    asm volatile("" : "+v"(W[8]), "+v"(W[9]), "+v"(W[10]), "+v"(W[11]), "+v"(W[12]), "+v"(W[13]), "+v"(W[14]), "+v"(W[15]));
    wait_ge(C_DONE + NC - 2, (unsigned)(TPP / NC + 3), 1);
    wait_ge(C_DONE + NC - 1, (unsigned)(TPP / NC + 3), 1);
    const LAS unsigned* sp2 = (const LAS unsigned*)(L8 + Q_OFF) + par * 16 + ((lane >> 3) & 1) * 8 + cw;
    const unsigned pd0 = sp2[0], pd1 = sp2[32];
    const u32x4 A0 = {pd0 & am[0], pd0 & am[1], pd0 & am[2], pd0 & am[3]}, A1 = {pd1 & am[0], pd1 & am[1], pd1 & am[2], pd1 & am[3]};
    if (cw < NC - 2) {
      u32x4 V[8];
      fetch(V, NTILES - NC + cw, (unsigned)(NTILES / NC));
      pv(V, TPP / NC - 1);
    }
    wait_tile(NTILES - 2, true);
    const u32x4 V0 = *(const LAS u32x4*)(L8 + RING_OFF + ((NTILES - 2) & (NS - 1)) * TILE_BYTES + cw * 1024 + lane * 16);
    acc = __builtin_amdgcn_mfma_f32_32x32x16_bf16(__builtin_bit_cast(bf16x8, A0), __builtin_bit_cast(bf16x8, V0), acc, 0, 0, 0);
    wait_tile(NTILES - 1, false);
    const u32x4 V1 = *(const LAS u32x4*)(L8 + RING_OFF + ((NTILES - 1) & (NS - 1)) * TILE_BYTES + cw * 1024 + lane * 16);
    acc = __builtin_amdgcn_mfma_f32_32x32x16_bf16(__builtin_bit_cast(bf16x8, A1), __builtin_bit_cast(bf16x8, V1), acc, 0, 0, 0);
    LAS float* rec = pb + c * 8 + par * 4;
    *(LAS f32x4*)rec = f32x4{acc[0], acc[1], acc[2], acc[3]};
    *(LAS f32x4*)(rec + 256) = f32x4{acc[4], acc[5], acc[6], acc[7]};
  }
  if (cw == 7) STAMP(9);
  BARRIER();
  if (cw == 0) STAMP(10);
  if (ct < 128) {
    f32x4 o = *(const LAS f32x4*)((const LAS float*)(L8 + PB_OFF) + ct * 4);
#pragma unroll
    for (int w = 1; w < NC; ++w) o += *(const LAS f32x4*)((const LAS float*)(L8 + PB_OFF) + w * 512 + ct * 4);
    __builtin_amdgcn_raw_buffer_store_b128(u32x4{nosent(o[0]), nosent(o[1]), nosent(o[2]), nosent(o[3])}, rs_ws, (int)WS_OB + bid * OB_BYTES + OB_O + ct * 16, 0, 16);
  }
  if (cw == 0) STAMP(25);
  const u32x4 rsv = {(unsigned)(size_t)a.ws, (unsigned)((size_t)a.ws >> 32) & 0xffffu, (unsigned)WS_END, 0x00020000u};
  {
    const int off = (int)WS_OB + (kvh + 8 * b + 64 * (lane >> 4)) * OB_BYTES + OB_O + (s * 128 + cw * 16 + (lane & 15)) * 4;
    unsigned ov = SENT;
    {
      const u64 t0 = rt(); unsigned sp = 0;
      if (CFG_H1D0) __builtin_amdgcn_s_sleep(CFG_H1D0);
      for (;;) {
        if (ov == SENT) ov = __builtin_amdgcn_raw_buffer_load_b32(rs_ws, off, 0, 16);
        if (__all(ov != SENT)) break;
        if (CFG_H1D1) __builtin_amdgcn_s_sleep(CFG_H1D1);
        sp += 15u;
        if (expired(t0, sp)) break;
      }
    }
    if (cw == 0) STAMP(11);
    float ot = __uint_as_float(ov);
    ot = swap16_add(ot, ot); ot = swap32_add(ot, ot);
    const float ob = rbf(ot);
    const unsigned opair = (__float_as_uint(ob) >> 16) | __float_as_uint(dpp<XOR1>(ob));
    float pa[8];
#pragma unroll
    for (int k = 0; k < 8; ++k) pa[k] = 0.f;
#pragma unroll
    for (int i = 0; i < 8; ++i) {
      const unsigned op = (unsigned)__builtin_amdgcn_readlane((int)opair, 2 * i);
#pragma unroll
      for (int k = 0; k < 4; ++k) { pa[2 * k] = dot2(W[2 * i][k], op, pa[2 * k]); pa[2 * k + 1] = dot2(W[2 * i + 1][k], op, pa[2 * k + 1]); }
    }
    if (lane < 56) {
      LAS float* red = (LAS float*)(L8 + RED_OFF) + cw * D + lane * 8;
      *(LAS f32x4*)red = f32x4{pa[0], pa[1], pa[2], pa[3]};
      *(LAS f32x4*)(red + 4) = f32x4{pa[4], pa[5], pa[6], pa[7]};
    }
  }
  if (cw == 0) STAMP(12);
  BARRIER();
  if (cw == 0) STAMP(26);
  if (ct < 112) {
    f32x4 v = *(const LAS f32x4*)((const LAS float*)(L8 + RED_OFF) + ct * 4);
#pragma unroll
    for (int w = 1; w < NC; ++w) v += *(const LAS f32x4*)((const LAS float*)(L8 + RED_OFF) + w * D + ct * 4);
    __builtin_amdgcn_raw_buffer_store_b128(u32x4{nosent(v[0]), nosent(v[1]), nosent(v[2]), nosent(v[3])}, rs_ws, (int)WS_OB + bid * OB_BYTES + OB_P + ct * 16, 0, 16);
  }
  if (cw == 0) STAMP(27);
  if (!is_final) { if (cw == 0) STAMP_FLUSH(); return; }
  if (cw == 7) return;
  {
    const int off = (int)WS_OB + (8 * b + 64 * (lane >> 4)) * OB_BYTES + OB_P + (cw * 64 + (lane & 15) * 4) * 4;
    u32x4 v[8];
#pragma unroll
    for (int i = 0; i < 8; ++i) v[i] = u32x4{SENT, SENT, SENT, SENT};
    {
      const u64 t0 = rt(); unsigned sp = 0;
      if (CFG_H2D0) __builtin_amdgcn_s_sleep(CFG_H2D0);
      for (;;) {
        bool ok = true;
#pragma unroll
        for (int i = 0; i < 8; ++i) if (!full4(v[i])) v[i] = __builtin_amdgcn_raw_buffer_load_b128(rs_ws, off + i * OB_BYTES, 0, 16);
#pragma unroll
        for (int i = 0; i < 8; ++i) ok &= full4(v[i]);
        if (__all(ok)) break;
        sp += 15u;
        if (expired(t0, sp)) break;
      }
    }
    if (cw == 0) STAMP(13);
    f32x4 sum = __builtin_bit_cast(f32x4, v[0]);
#pragma unroll
    for (int i = 1; i < 8; ++i) sum += __builtin_bit_cast(f32x4, v[i]);
    const float e0 = swap32_add(sum[0], sum[2]), e1 = swap32_add(sum[1], sum[3]);
    const float att = swap16_add(e0, e1);
    const float h = rbf(bf_lo(xres) + rbf(att));
    float t = h;
#pragma unroll
    for (int r = 0; r < 2; ++r) {
      const float cs = first_lane(t), d = t - cs;
      const float s1 = wave_sum(d), s2 = wave_sum(d * d);
      LAS float* ex = (LAS float*)(L8 + EX_OFF) + r * 16;
      if (lane == 0) { ex[cw * 2] = fmaf(s1, 1.f / 64.f, cs); ex[cw * 2 + 1] = fmaxf(fmaf(-s1 * (1.f / 64.f), s1, s2), 0.f); }
      BARRIER();
      if (cw == 0) STAMP(22 + r);
      const f32x4 x0 = *(const LAS f32x4*)ex, x1 = *(const LAS f32x4*)(ex + 4), x2 = *(const LAS f32x4*)(ex + 8);
      const float m6 = ex[12], q6 = ex[13];
      const float mean = (((x0[0] + x0[2]) + (x1[0] + x1[2])) + ((x2[0] + x2[2]) + m6)) * (1.f / 7.f);
      float dv = 0.f;
      { const float d0 = x0[0] - mean, d1 = x0[2] - mean, d2 = x1[0] - mean, d3 = x1[2] - mean, d4 = x2[0] - mean, d5 = x2[2] - mean, d6 = m6 - mean;
        dv = ((d0 * d0 + d1 * d1) + (d2 * d2 + d3 * d3)) + ((d4 * d4 + d5 * d5) + d6 * d6); }
      const float m2 = (((x0[1] + x0[3]) + (x1[1] + x1[3])) + ((x2[1] + x2[3]) + q6)) + 64.f * dv;
      t = ln_apply(t, mean, m2 * (1.f / D));
    }
    const unsigned bits = __float_as_uint(t) >> 16;
    const unsigned e = ((bits >> 7) & 0xffu) - (unsigned)E0;
    const bool miss = e >= (unsigned)NE;
    float y = bf1(((const LAS u16*)(L8 + TAB_OFF))[(((bits >> 15) << 7) | (bits & 127u)) * NE + (miss ? 0u : e)]);
    u64 mm = __ballot(miss);
    while (mm) {
      const int src = __builtin_ctzll(mm); mm &= mm - 1;
      const float tv = __builtin_bit_cast(float, __builtin_amdgcn_readlane(__builtin_bit_cast(int, t), src));
      float ya = 0.f;
#pragma unroll 2
      for (int f = lane; f < F; f += 64) ya = fmaf(mixer_unit(tv, bf1(((const GAS u16*)a.w1)[f]), bf1(((const GAS u16*)a.b1)[f])), bf1(((const GAS u16*)a.w2)[f]), ya);
      const float yv = rbf(rbf(wave_sum(ya)) + bf1(*(const GAS u16*)a.b2));
      if (lane == src) y = yv;
    }
    ((GAS u16*)a.out)[b * D + fcol] = (u16)bfbits(h + y);
    if (b == 0 && ct == 0) __hip_atomic_store((GAS u64*)(ws + WS_EPOCH), ((u64)ctl[C_EP] | ((u64)ctl[C_EP + 1] << 32)) + 1ull, RLX_AGENT);
    if (cw == 0) { STAMP(14); STAMP_FLUSH(); }
  }
}
}

extern "C" void kernel_launch(void* const* d_in, const int* in_sizes, int n_in, void* d_out, int out_size, void* d_ws, size_t ws_size,
                              hipStream_t stream) {
  Args a;
  a.x = (const u16*)d_in[0]; a.kc = (const u16*)d_in[1]; a.vc = (const u16*)d_in[2]; a.wq = (const u16*)d_in[3]; a.wo = (const u16*)d_in[4];
  a.w1 = (const u16*)d_in[6]; a.b1 = (const u16*)d_in[7]; a.w2 = (const u16*)d_in[8]; a.b2 = (const u16*)d_in[9];
  a.out = (u16*)d_out; a.ws = (unsigned char*)d_ws;
  (void)hipFuncSetAttribute((const void*)k_fused, hipFuncAttributeMaxDynamicSharedMemorySize, LDS_BYTES);
  k_fused<<<NWG, NTHR, LDS_BYTES, stream>>>(a);
}
```

```cpp
#include <hip/hip_runtime.h>
#include <hip/hip_bf16.h>
#include <cstdint>

namespace {
#define GAS __attribute__((address_space(1)))
#define LAS __attribute__((address_space(3)))
#define RLX_AGENT __ATOMIC_RELAXED, __HIP_MEMORY_SCOPE_AGENT
typedef unsigned u32x4 __attribute__((ext_vector_type(4)));
typedef unsigned u32x2 __attribute__((ext_vector_type(2)));
typedef float f32x4 __attribute__((ext_vector_type(4)));
typedef float f32x16 __attribute__((ext_vector_type(16)));
typedef short bf16x8 __attribute__((ext_vector_type(8)));
typedef __bf16 bf16x2 __attribute__((ext_vector_type(2)));
typedef unsigned long long u64;
typedef unsigned short u16;

constexpr int B = 8, D = 448, HQ = 16, HKV = 8, SK = 8192, DH = 256, F = 1792;
constexpr int NWG = 256, NL = 2, NC = 8, NWAVES = NL + NC + 2, NTHR = NWAVES * 64, SVC = NL + NC, PACER = SVC + 1;
constexpr int TILE_BYTES = 8192, NS = 16;
constexpr int TPP = 128, NTILES = 2 * TPP;
constexpr int WIN = 12, WMIN = WIN - 3, WMAX = WIN + 4;
constexpr int CKSTEP = 32, NCK = (NTILES - 1) / CKSTEP;
constexpr int PDELAY = 250;
constexpr float PGAIN = 0.06f;
constexpr int WJ0 = 6;
constexpr float EPS = 1e-5f;
#define MIXER_NE 24
constexpr int NE = MIXER_NE, E0 = 132 - NE;
static_assert((256 * NE) % 512 == 0 && NE <= 24, "table shape");

constexpr int RING_OFF = 0;
constexpr int PB_OFF = NS * TILE_BYTES;
constexpr int TAB_OFF = PB_OFF + 16384;
constexpr int Q_OFF = TAB_OFF + 256 * 24 * 2;
constexpr int CTL_OFF = Q_OFF + 1024;
constexpr int ML_OFF = CTL_OFF + 256;
constexpr int GS_OFF = ML_OFF + 128;
constexpr int LDS_BYTES = GS_OFF + 64;
static_assert(LDS_BYTES <= 160 * 1024 && LDS_BYTES > 80 * 1024, "one workgroup per CU");
constexpr int C_LANDED = 0  , C_WQ = 2, C_TICKET = 3, C_DONE = 4  , C_QRDY = 12, C_PRO = 13, C_SCALE = 14, C_ABORT = 15, C_ML = 16,
              C_WIN = 17, C_CKN = 18, C_EPF = 19, C_EP = 20  , C_CKT = 32  ;
constexpr int RED_OFF = 0;
constexpr int EX_OFF = 16384;

constexpr size_t WS_EPOCH = 0;
constexpr size_t WS_READY = 4096;
constexpr size_t WS_QG = 65536;
constexpr size_t WS_TABG = WS_QG + (size_t)B * HQ * DH * 8;
constexpr size_t WS_PROG = WS_TABG + 65536;
constexpr size_t WS_OB = WS_PROG + 32768;
constexpr int OB_BYTES = 4096, OB_O = 0, OB_P = 2048, OB_ML = 3840;
constexpr int WS_END = (int)(WS_OB + (size_t)NWG * OB_BYTES);
constexpr unsigned SENT = 0xFFFFFFFFu;
constexpr u64 TAGM = 0xFFFFFFFFFFFFull;
constexpr long long TCAP = 5000000;

__device__ __forceinline__ float bf_lo(unsigned u) { return __builtin_bit_cast(float, u << 16); }
__device__ __forceinline__ float bf_hi(unsigned u) { return __builtin_bit_cast(float, u & 0xffff0000u); }
__device__ __forceinline__ float bf1(u16 u) { return __builtin_bit_cast(float, (unsigned)u << 16); }
__device__ __forceinline__ float rbf(float x) { return (float)(__bf16)x; }
__device__ __forceinline__ unsigned bfbits(float x) { return (unsigned)__builtin_bit_cast(u16, (__bf16)x); }
__device__ __forceinline__ float dot2(unsigned a, unsigned b, float acc) {
  return __builtin_amdgcn_fdot2_f32_bf16(__builtin_bit_cast(bf16x2, a), __builtin_bit_cast(bf16x2, b), acc, false);
}
template <int CTRL> __device__ __forceinline__ float dpp(float x) {
  return __builtin_bit_cast(float, __builtin_amdgcn_mov_dpp(__builtin_bit_cast(int, x), CTRL, 0xf, 0xf, true));
}
constexpr int XOR1 = 0xB1, XOR2 = 0x4E, XOR7 = 0x141, ROR8 = 0x128, ROR4 = 0x124;
__device__ __forceinline__ float swap16_add(float a, float b) {
  auto r = __builtin_amdgcn_permlane16_swap(__float_as_uint(a), __float_as_uint(b), false, false);
  return __uint_as_float(r[0]) + __uint_as_float(r[1]);
}
__device__ __forceinline__ float swap32_add(float a, float b) {
  auto r = __builtin_amdgcn_permlane32_swap(__float_as_uint(a), __float_as_uint(b), false, false);
  return __uint_as_float(r[0]) + __uint_as_float(r[1]);
}
__device__ __forceinline__ float swap16_max(float a) {
  auto r = __builtin_amdgcn_permlane16_swap(__float_as_uint(a), __float_as_uint(a), false, false);
  return fmaxf(__uint_as_float(r[0]), __uint_as_float(r[1]));
}
__device__ __forceinline__ float swap32_max(float a) {
  auto r = __builtin_amdgcn_permlane32_swap(__float_as_uint(a), __float_as_uint(a), false, false);
  return fmaxf(__uint_as_float(r[0]), __uint_as_float(r[1]));
}
__device__ __forceinline__ float wave_sum(float x) {
  x += dpp<XOR1>(x); x += dpp<XOR2>(x); x += dpp<XOR7>(x); x += dpp<ROR8>(x);
  x = swap16_add(x, x);
  return swap32_add(x, x);
}
__device__ __forceinline__ float first_lane(float v) { return __builtin_bit_cast(float, __builtin_amdgcn_readfirstlane(__builtin_bit_cast(int, v))); }
__device__ __forceinline__ float fdiv(float a, float b, float rb) {
  const float q = a * rb;
  return fmaf(fmaf(-q, b, a), rb, q);
}
__device__ __forceinline__ float ln_apply(float x, float mean, float var) {
  const float m = rbf(mean);
  const float den = rbf(__builtin_amdgcn_sqrtf(rbf(rbf(var) + EPS)));
  return rbf(fdiv(rbf(x - m), den, __builtin_amdgcn_rcpf(den)));
}
__device__ __forceinline__ void ln448(float (&v)[7]) {
  const float c = first_lane(v[0]);
  float s1 = 0.f, s2 = 0.f;
#pragma unroll
  for (int i = 0; i < 7; ++i) { const float d = v[i] - c; s1 += d; s2 = fmaf(d, d, s2); }
  s1 = wave_sum(s1) * (1.f / D);
  s2 = wave_sum(s2) * (1.f / D);
  const float var = fmaxf(fmaf(-s1, s1, s2), 0.f), mean = c + s1;
#pragma unroll
  for (int i = 0; i < 7; ++i) v[i] = ln_apply(v[i], mean, var);
}
__device__ __forceinline__ float mixer_unit(float t, float w1, float b1) {
  constexpr float K0 = -2.f * 0.7978845608028654f * 1.4426950408889634f, K1 = K0 * 0.044715f;
  const float u = rbf(rbf(t * w1) + b1);
  const float z = u * fmaf(u * u, K1, K0);
  return rbf(u * __builtin_amdgcn_rcpf(1.f + __builtin_amdgcn_exp2f(z)));
}
__device__ __forceinline__ u64 rt() { return __builtin_amdgcn_s_memrealtime(); }
#define LDS_WAIT() asm volatile("s_waitcnt lgkmcnt(0)" ::: "memory")
#define DRAIN() asm volatile("s_waitcnt vmcnt(0)" ::: "memory")
#define BARRIER() asm volatile("s_waitcnt lgkmcnt(0)\n\ts_barrier" ::: "memory")
#define GLDS(src, dst) __builtin_amdgcn_global_load_lds((const GAS void*)(src), (LAS void*)(dst), 16, 0, 2)
__device__ __forceinline__ unsigned nosent(float v) { const unsigned u = __float_as_uint(v); return u == SENT ? 0x7FC00000u : u; }
__device__ __forceinline__ bool full4(u32x4 v) { return max(max(v[0], v[1]), max(v[2], v[3])) != SENT; }

struct Args {
  const u16* x; const u16* kc; const u16* vc; const u16* wq; const u16* wo;
  const u16* w1; const u16* b1; const u16* w2; const u16* b2;
  u16* out; unsigned char* ws;
};

__global__ void __launch_bounds__(NTHR, 3) k_fused(Args a) {
  extern __shared__ __attribute__((aligned(16))) unsigned char smem[];
  LAS unsigned char* L8 = (LAS unsigned char*)smem;
  volatile LAS unsigned* ctl = (volatile LAS unsigned*)(L8 + CTL_OFF);
  const int tid = threadIdx.x, lane = tid & 63, wave = __builtin_amdgcn_readfirstlane(tid >> 6);
  const int bid = blockIdx.x, kvh = bid & 7, b = (bid >> 3) & 7, s = bid >> 6;
  const bool is_final = (kvh == b) && (s == 0);
  GAS unsigned char* ws = (GAS unsigned char*)a.ws;
  const size_t chunk = ((size_t)(b * HKV + kvh) * SK + (size_t)s * (SK / 4)) * (DH * 2);
  const auto rs_ws = __builtin_amdgcn_make_buffer_rsrc((void*)a.ws, 0, WS_END, 0x00020000);
  auto expired = [&](u64 t0, unsigned& sp) -> bool {
    if ((++sp & 255u) != 0u) return false;
    if (ctl[C_ABORT] != 0u) return true;
    if ((long long)(rt() - t0) > TCAP) { ctl[C_ABORT] = 1u; return true; }
    return false;
  };
  auto wait_ge = [&](int word, unsigned need, int nap) {
    if (ctl[word] < need) {
      const u64 t0 = rt(); unsigned sp = 0;
      do { if (nap == 1) __builtin_amdgcn_s_sleep(1); else if (nap > 1) __builtin_amdgcn_s_sleep(8); if (expired(t0, sp)) break; } while (ctl[word] < need);
    }
    asm volatile("" ::: "memory");
  };
  auto lds_inc = [&](int word) {
    if (lane == 0) __hip_atomic_fetch_add((LAS unsigned*)(L8 + CTL_OFF) + word, 1u, __ATOMIC_RELAXED, __HIP_MEMORY_SCOPE_WORKGROUP);
  };
  auto uniform64 = [](u64 v) -> u64 {
    return (u64)(unsigned)__builtin_amdgcn_readfirstlane((int)(unsigned)v) | ((u64)(unsigned)__builtin_amdgcn_readfirstlane((int)(unsigned)(v >> 32)) << 32);
  };
  auto get_epoch1 = [&]() -> u64 {
    wait_ge(C_EPF, 1u, 0);
    return ((u64)ctl[C_EP] | ((u64)ctl[C_EP + 1] << 32)) + 1ull;
  };
  constexpr float C2 = 0.0625f * 1.4426950408889634f;

  if (__builtin_expect(wave < NL, 1)) {
    const int L = wave;
    __builtin_amdgcn_s_setprio(3);
    const int rot = ((bid >> 6) & 1) * 16 + ((bid >> 5) & 1) * 32;
    const GAS unsigned char* kbase = (const GAS unsigned char*)a.kc + chunk;
    const unsigned vo = lane * 16;
#pragma unroll 1
    for (int j = 0; j < 8; ++j) GLDS(kbase + (rot + L) * TILE_BYTES + j * 1024 + vo, L8 + RING_OFF + L * TILE_BYTES + j * 1024);
    BARRIER();
    const GAS unsigned char* vbase = (const GAS unsigned char*)a.vc + chunk;
    auto draw = [&]() -> unsigned {
      unsigned t; u64 keep;
      asm volatile("s_mov_b64 %1, exec\n\ts_mov_b64 exec, 1\n\tds_add_rtn_u32 %0, %2, %3\n\ts_mov_b64 exec, %1"
                   : "=&v"(t), "=&s"(keep) : "v"((unsigned)(size_t)(L8 + CTL_OFF + C_TICKET * 4)), "v"(1u) : "memory");
      return t;
    };
    bool dead = false;
    int pub = 0, h1 = L, h2 = 0;
    unsigned cv = 0, nxt = draw();
    for (;;) {
      asm volatile("s_waitcnt lgkmcnt(0)" : "+v"(nxt)::"memory");
      const int T = NL + __builtin_amdgcn_readfirstlane((int)nxt);
      if (T >= NTILES) break;
      if (T >= NS) {
        const unsigned need = (unsigned)((T - NS) / NC + 1);
        if ((unsigned)__builtin_amdgcn_readlane((int)cv, C_DONE + (T & (NC - 1))) < need && ctl[C_DONE + (T & (NC - 1))] < need) {
          DRAIN();
          pub = T;
          ctl[C_LANDED + L] = (unsigned)pub;
          const u64 t0 = rt(); unsigned sp = 0;
          while (ctl[C_DONE + (T & (NC - 1))] < need) { __builtin_amdgcn_s_sleep(1); if (expired(t0, sp)) { dead = true; break; } }
          if (dead) break;
        }
        asm volatile("" ::: "memory");
      }
      const int wnow = (__builtin_amdgcn_readlane((int)cv, C_WIN) + L) >> 1;
      LAS unsigned char* dst = L8 + RING_OFF + (T & (NS - 1)) * TILE_BYTES;
      const GAS unsigned char* ub = (T < TPP ? kbase : vbase) + ((T + rot) & (TPP - 1)) * TILE_BYTES;
#define PIECE(p) __builtin_amdgcn_global_load_lds((const GAS void*)(ub + ((p) >> 2) * 4096 + vo), (LAS void*)(dst + ((p) >> 2) * 4096), 16, ((p) & 3) * 1024, 2)
#define STEP(Wn, j)                                                                                     \
        PIECE(j); PIECE(j + 1);                                                                         \
        if (j == 6) { nxt = draw(); cv = ctl[lane & 31]; }                                              \
        asm volatile("s_waitcnt vmcnt(%0)" ::"i"(Wn) : "memory");                                       \
        if ((j + 34 - (Wn)) % 8 < 2) {                   \
          constexpr int back = 4 - (j + 34 - (Wn)) / 8;     \
          const int v = back <= 0 ? T : back == 1 ? h1 : h2;                                            \
          if (v > pub) { pub = v; ctl[C_LANDED + L] = (unsigned)pub; }                                  \
        }
#define TILE_BODY(Wn) { STEP(Wn, 0) STEP(Wn, 2) STEP(Wn, 4) STEP(Wn, 6) }
      switch (wnow) {
        case 1: TILE_BODY(WMIN) break;
        case 2: TILE_BODY(WMIN + 1) break;
        case 3: TILE_BODY(WMIN + 2) break;
        case 5: TILE_BODY(WMIN + 4) break;
        case 6: TILE_BODY(WMIN + 5) break;
        case 7: TILE_BODY(WMIN + 6) break;
        case 8: TILE_BODY(WMIN + 7) break;
        default: TILE_BODY(WIN) break;
      }
      h2 = h1; h1 = T;
      if ((T % CKSTEP) == 0) { ctl[C_CKT + T / CKSTEP - 1] = (unsigned)rt(); ctl[C_CKN] = (unsigned)(T / CKSTEP); }
    }
    if (!dead) {
      asm volatile("s_waitcnt vmcnt(16)" ::: "memory");
      if (h2 > pub) ctl[C_LANDED + L] = (unsigned)h2;
      asm volatile("s_waitcnt vmcnt(8)" ::: "memory");
      if (h1 > pub) ctl[C_LANDED + L] = (unsigned)h1;
      DRAIN();
      ctl[C_LANDED + L] = (unsigned)NTILES;
    } else {
      DRAIN();
    }
    LDS_WAIT();
    return;
  }

  if (wave == PACER) {
    BARRIER();
    const u64 ep1 = uniform64(get_epoch1()), tag = ep1 & TAGM;
#pragma unroll 1
    for (int c = 1; c <= NCK; ++c) {
      if (c == TPP / CKSTEP) {
        const u64 t0 = rt(); unsigned sp = 0;
        const int pk = is_final ? (lane & 7) : kvh, ps = (lane >> 3) & 3;
        const GAS u64* rf = (const GAS u64*)(ws + WS_READY) + (pk + 8 * b + 64 * ps);
        for (;;) {
          const u64 v = __hip_atomic_load(rf, RLX_AGENT);
          if (__all(v == ep1)) break;
          __builtin_amdgcn_s_sleep(16);
          sp += 15u;
          if (expired(t0, sp)) break;
        }
      }
      wait_ge(C_CKN, (unsigned)c, c == TPP / CKSTEP ? 1 : 2);
      const unsigned tc = ctl[C_CKT + c - 1];
      GAS u64* pg = (GAS u64*)(ws + WS_PROG) + (c - 1) * NWG;
      if (lane == 0) __hip_atomic_store(pg + bid, (tag << 16) | (u64)(tc & 0xffffu), RLX_AGENT);
      if (c == TPP / CKSTEP) {
        const u64 t0 = rt(); unsigned sp = 0;
        wait_ge(C_ML, (unsigned)NC, 1);
        const LAS float* ml = (const LAS float*)(L8 + ML_OFF) + (lane & 15) * 2;
        const float mw = ml[0], lw = ml[1];
        float M = fmaxf(mw, dpp<XOR2>(mw)); M = fmaxf(M, dpp<ROR4>(M)); M = fmaxf(M, dpp<ROR8>(M));
        float Ls = lw * __builtin_amdgcn_exp2f((mw - M) * C2);
        Ls += dpp<XOR2>(Ls); Ls += dpp<ROR4>(Ls); Ls += dpp<ROR8>(Ls);
        if (lane < 2) __builtin_amdgcn_raw_buffer_store_b64(u32x2{nosent(M), nosent(Ls)}, rs_ws, (int)WS_OB + bid * OB_BYTES + OB_ML + lane * 8, 0, 16);
        const int off = (int)WS_OB + (kvh + 8 * b + 64 * (lane >> 4)) * OB_BYTES + OB_ML + (lane & 1) * 8;
        u32x2 v = {SENT, SENT};
        __builtin_amdgcn_s_sleep(32);
        for (;;) {
          __builtin_amdgcn_s_sleep(8);
          if (v[0] == SENT || v[1] == SENT) v = __builtin_amdgcn_raw_buffer_load_b64(rs_ws, off, 0, 16);
          if (__all(v[0] != SENT && v[1] != SENT)) break;
          sp += 15u;
          if (expired(t0, sp)) break;
        }
        const float Ms = __uint_as_float(v[0]);
        const float Mg = swap32_max(swap16_max(Ms));
        float Lg = __uint_as_float(v[1]) * __builtin_amdgcn_exp2f((Ms - Mg) * C2);
        Lg = swap16_add(Lg, Lg); Lg = swap32_add(Lg, Lg);
        if (lane < 2) { LAS float* gs = (LAS float*)(L8 + GS_OFF) + lane * 2; gs[0] = Mg; gs[1] = 1.f / Lg; }
        LDS_WAIT();
        ctl[C_SCALE] = 1u;
      }
      while ((unsigned)rt() - tc < (unsigned)PDELAY) __builtin_amdgcn_s_sleep(16);
      const u64 g = __hip_atomic_load(pg + (bid & ~63) + lane, RLX_AGENT);
      const int d = (int)(short)(u16)(tc - (unsigned)g);
      const float df = ((g >> 16) == tag) ? (float)min(max(d, -(PDELAY - 100)), 600) : -(float)(PDELAY - 100);
      const float lag = wave_sum(df) * (1.f / 64.f);
      const int w = min(max(2 * WIN + (int)rintf(lag * PGAIN), 2 * WMIN), 2 * WMAX);
      ctl[C_WIN] = (unsigned)(w - 2 * WMIN + 2);
    }
    LDS_WAIT();
    return;
  }

  if (wave == SVC) {
    u64 epoch = __hip_atomic_load((GAS u64*)(ws + WS_EPOCH), RLX_AGENT);
    ((LAS unsigned*)(L8 + CTL_OFF))[lane] = 0u;
    BARRIER();
    asm volatile("" : "+v"(epoch));
    epoch = uniform64(epoch);
    ctl[C_EP] = (unsigned)epoch; ctl[C_EP + 1] = (unsigned)(epoch >> 32);
    LDS_WAIT();
    ctl[C_EPF] = 1u;
    const u64 tag = (epoch + 1ull) & TAGM;
    {
      const GAS u64* qg = (const GAS u64*)(ws + WS_QG) + b * (HQ * DH) + kvh * 512 + lane;
      u64 g[8];
#pragma unroll
      for (int i = 0; i < 8; ++i) g[i] = ~tag << 16;
      wait_ge(C_PRO, (unsigned)NC, 1);
      const u64 t0 = rt(); unsigned sp = 0;
      while ((long long)(rt() - t0) < 220) __builtin_amdgcn_s_sleep(4);
      for (;;) {
        bool ok = true;
#pragma unroll
        for (int i = 0; i < 8; ++i) if ((g[i] >> 16) != tag) g[i] = __hip_atomic_load(qg + i * 64, RLX_AGENT);
#pragma unroll
        for (int i = 0; i < 8; ++i) ok &= ((g[i] >> 16) == tag);
        if (__all(ok)) break;
        __builtin_amdgcn_s_sleep(32);
        sp += 15u;
        if (expired(t0, sp)) break;
      }
#pragma unroll
      for (int i = 0; i < 8; ++i) ((LAS u16*)(L8 + Q_OFF))[i * 64 + lane] = (u16)g[i];
      LDS_WAIT();
      ctl[C_QRDY] = 1u;
    }
#pragma unroll
    for (int i = 0; i < OB_BYTES / 1024; ++i)
      __builtin_amdgcn_raw_buffer_store_b128(u32x4{SENT, SENT, SENT, SENT}, rs_ws, (int)WS_OB + bid * OB_BYTES + i * 1024 + lane * 16, 0, 16);
    unsigned pw1[14], pb1[14], pw2[14];
#pragma unroll
    for (int k = 0; k < 14; ++k) {
      pw1[k] = __builtin_nontemporal_load((const GAS unsigned*)a.w1 + lane + 64 * k);
      pb1[k] = __builtin_nontemporal_load((const GAS unsigned*)a.b1 + lane + 64 * k);
      pw2[k] = __builtin_nontemporal_load((const GAS unsigned*)a.w2 + lane + 64 * k);
    }
    const float b2 = bf1(__builtin_nontemporal_load((const GAS u16*)a.b2));
    DRAIN();
    if (lane == 0) __hip_atomic_store((GAS u64*)(ws + WS_READY) + bid, epoch + 1ull, RLX_AGENT);
    {
      unsigned mine = 0;
#pragma unroll 1
      for (int e = 0; e < NE; ++e) {
        const float t = bf1((u16)(((unsigned)(bid >> 7) << 15) | ((unsigned)(E0 + e) << 7) | (unsigned)(bid & 127)));
        float acc0 = 0.f, acc1 = 0.f;
#pragma unroll
        for (int k = 0; k < 14; ++k) {
          acc0 = fmaf(mixer_unit(t, bf_lo(pw1[k]), bf_lo(pb1[k])), bf_lo(pw2[k]), acc0);
          acc1 = fmaf(mixer_unit(t, bf_hi(pw1[k]), bf_hi(pb1[k])), bf_hi(pw2[k]), acc1);
        }
        const float y = rbf(rbf(wave_sum(acc0 + acc1)) + b2);
        if (lane == e) mine = bfbits(y);
      }
      if (lane < NE) __hip_atomic_store((GAS u64*)(ws + WS_TABG) + bid * NE + lane, (tag << 16) | (u64)mine, RLX_AGENT);
    }
    if (is_final) {
      const GAS u64* tg = (const GAS u64*)(ws + WS_TABG) + lane;
      const u64 t0 = rt(); unsigned sp = 0;
      while ((long long)(rt() - t0) < 400) __builtin_amdgcn_s_sleep(32);
#pragma unroll 1
      for (int c = 0; c < 256 * NE / 512; ++c) {
        for (;;) {
          u64 g[8]; bool ok = true;
#pragma unroll
          for (int i = 0; i < 8; ++i) g[i] = __hip_atomic_load(tg + c * 512 + i * 64, RLX_AGENT);
#pragma unroll
          for (int i = 0; i < 8; ++i) { ok &= ((g[i] >> 16) == tag); ((LAS u16*)(L8 + TAB_OFF))[c * 512 + i * 64 + lane] = (u16)g[i]; }
          if (__all(ok)) break;
          __builtin_amdgcn_s_sleep(32);
          sp += 15u;
          if (expired(t0, sp)) break;
        }
      }
    }
    LDS_WAIT();
    return;
  }

  const int cw = wave - NL, ct = tid - NL * 64;
  const int par = lane >> 5, c = lane & 31;
  {
    unsigned xr[7];
#pragma unroll
    for (int i = 0; i < 7; ++i) xr[i] = __builtin_nontemporal_load((const GAS u16*)a.x + cw * D + lane + 64 * i);
    if (cw < 7) {
      const GAS unsigned char* wsrc = (const GAS unsigned char*)a.wq + (size_t)(cw * 64 + (lane >> 1)) * (HQ * DH * 2) + (kvh * 512 + (bid >> 3) * 16) * 2 + (lane & 1) * 16;
      GLDS(wsrc, L8 + PB_OFF + cw * 2048);
      GLDS(wsrc + (size_t)32 * (HQ * DH * 2), L8 + PB_OFF + cw * 2048 + 1024);
    }
    BARRIER();
    DRAIN();
    lds_inc(C_WQ);
    asm volatile("" : "+v"(xr[0]), "+v"(xr[1]), "+v"(xr[2]), "+v"(xr[3]), "+v"(xr[4]), "+v"(xr[5]), "+v"(xr[6]));
    float xn[7];
#pragma unroll
    for (int i = 0; i < 7; ++i) xn[i] = bf_lo(xr[i]);
    ln448(xn);
    wait_ge(C_WQ, (unsigned)NC, 0);
    float acc[16];
#pragma unroll
    for (int k = 0; k < 16; ++k) acc[k] = 0.f;
#pragma unroll
    for (int i = 0; i < 7; ++i) {
      const LAS u32x4* wp = (const LAS u32x4*)(L8 + PB_OFF + (lane + 64 * i) * 32);
      const u32x4 w0 = wp[0], w1 = wp[1];
      const unsigned xhi = __float_as_uint(xn[i]), xlo = xhi >> 16;
#pragma unroll
      for (int k = 0; k < 4; ++k) {
        acc[2 * k] = dot2(w0[k], xlo, acc[2 * k]); acc[2 * k + 1] = dot2(w0[k], xhi, acc[2 * k + 1]);
        acc[8 + 2 * k] = dot2(w1[k], xlo, acc[8 + 2 * k]); acc[8 + 2 * k + 1] = dot2(w1[k], xhi, acc[8 + 2 * k + 1]);
      }
    }
    LDS_WAIT();
    lds_inc(C_PRO);
#pragma unroll
    for (int j = 0; j < 8; ++j) acc[j] = swap32_add(acc[j], acc[j + 8]);
#pragma unroll
    for (int j = 0; j < 4; ++j) acc[j] = swap16_add(acc[j], acc[j + 4]);
#pragma unroll
    for (int j = 0; j < 2; ++j) { const float t1 = acc[j] + dpp<ROR8>(acc[j]), t2 = acc[j + 2] + dpp<ROR8>(acc[j + 2]); acc[j] = (lane & 8) ? t2 : t1; }
    { const float t1 = acc[0] + dpp<XOR7>(acc[0]), t2 = acc[1] + dpp<XOR7>(acc[1]); acc[0] = (lane & 4) ? t2 : t1; }
    float qv = acc[0] + dpp<XOR2>(acc[0]);
    qv += dpp<XOR1>(qv);
    const u64 tag = get_epoch1() & TAGM;
    if ((lane & 3) == 0)
      __hip_atomic_store((GAS u64*)(ws + WS_QG) + cw * (HQ * DH) + kvh * 512 + (bid >> 3) * 16 + (lane >> 2), (tag << 16) | (u64)bfbits(qv), RLX_AGENT);
  }
  LAS float* pb = (LAS float*)(L8 + PB_OFF) + cw * 512;
  auto wait_tile = [&](int T, bool nap) {
    const volatile LAS u64* lp = (const volatile LAS u64*)(L8 + CTL_OFF + C_LANDED * 4);
    u64 v = *lp;
    if ((unsigned)v <= (unsigned)T || (unsigned)(v >> 32) <= (unsigned)T) {
      const u64 t0 = rt(); unsigned sp = 0;
      do { if (nap) __builtin_amdgcn_s_sleep(1); v = *lp; if (expired(t0, sp)) break; } while ((unsigned)v <= (unsigned)T || (unsigned)(v >> 32) <= (unsigned)T);
    }
    asm volatile("" ::: "memory");
  };
  auto fetch = [&](u32x4 (&R)[8], int T, unsigned n) {
    wait_tile(T, true);
    const LAS u32x4* tp = (const LAS u32x4*)(L8 + RING_OFF + (T & (NS - 1)) * TILE_BYTES) + lane;
#pragma unroll
    for (int i = 0; i < 8; ++i) R[i] = tp[i * 64];
    LDS_WAIT();
    ctl[C_DONE + cw] = n;
    asm volatile("" : "+v"(R[0]), "+v"(R[1]), "+v"(R[2]), "+v"(R[3]), "+v"(R[4]), "+v"(R[5]), "+v"(R[6]), "+v"(R[7])::"memory");
  };
  const int hs = (lane >> 1) & 1;
  const int spos = par * 16 + hs * 8 + ((lane >> 2) & 7);
  f32x16 acc;
#pragma unroll
  for (int k = 0; k < 16; ++k) acc[k] = 0.f;
  unsigned am[4];
  {
    const int j0 = lane & 7;
#pragma unroll
    for (int d = 0; d < 4; ++d) am[d] = (c < 16 && (j0 >> 1) == d) ? ((j0 & 1) ? 0xffff0000u : 0x0000ffffu) : 0u;
  }
  auto pv1 = [&](unsigned pd, u32x4 V) {
    const u32x4 A = {pd & am[0], pd & am[1], pd & am[2], pd & am[3]};
    acc = __builtin_amdgcn_mfma_f32_32x32x16_bf16(__builtin_bit_cast(bf16x8, A), __builtin_bit_cast(bf16x8, V), acc, 0, 0, 0);
  };
  auto pv = [&](const u32x4 (&V)[8], int j) {
    const LAS u32x4* pp = (const LAS u32x4*)(pb + j * 32 + par * 16 + ((lane >> 3) & 1) * 8);
    const u32x4 p0 = pp[0], p1 = pp[1];
#pragma unroll
    for (int i = 0; i < 8; ++i) pv1(i < 4 ? p0[i & 3] : p1[i & 3], V[i]);
  };
  {
    u32x4 Ra[8], Rb[8];
    fetch(Ra, cw, 1u);
    fetch(Rb, cw + NC, 2u);
    wait_ge(C_QRDY, 1u, 1);
    const u32x4 q0 = *(const LAS u32x4*)(L8 + Q_OFF + c * 16), q1 = *(const LAS u32x4*)(L8 + Q_OFF + 512 + c * 16);
    float mx = -INFINITY;
    auto score = [&](const u32x4 (&K)[8], int j) {
      float x[16];
#pragma unroll
      for (int i = 0; i < 8; ++i) {
        float d0 = 0.f, d1 = 0.f;
#pragma unroll
        for (int k = 0; k < 4; ++k) { d0 = dot2(K[i][k], q0[k], d0); d1 = dot2(K[i][k], q1[k], d1); }
        x[2 * i] = d0; x[2 * i + 1] = d1;
      }
#pragma unroll
      for (int u = 0; u < 8; ++u) x[u] = swap16_add(x[u], x[u + 8]);
#pragma unroll
      for (int u = 0; u < 4; ++u) { const float t1 = x[u] + dpp<ROR8>(x[u]), t2 = x[u + 4] + dpp<ROR8>(x[u + 4]); x[u] = (lane & 8) ? t2 : t1; }
#pragma unroll
      for (int u = 0; u < 2; ++u) { const float t1 = x[u] + dpp<XOR7>(x[u]), t2 = x[u + 2] + dpp<XOR7>(x[u + 2]); x[u] = (lane & 4) ? t2 : t1; }
      { const float t1 = x[0] + dpp<XOR2>(x[0]), t2 = x[1] + dpp<XOR2>(x[1]); x[0] = (lane & 2) ? t2 : t1; }
      const float sc = rbf(x[0] + dpp<XOR1>(x[0]));
      mx = fmaxf(mx, sc);
      pb[j * 32 + spos] = sc;
    };
#pragma unroll 1
    for (int j = 0; j < TPP / NC; j += 2) {
      score(Ra, j);
      if (j + 2 < TPP / NC) fetch(Ra, cw + NC * (j + 2), (unsigned)(j + 3));
      score(Rb, j + 1);
      if (j + 2 < TPP / NC) fetch(Rb, cw + NC * (j + 3), (unsigned)(j + 4));
    }
    mx = fmaxf(mx, dpp<ROR4>(mx)); mx = fmaxf(mx, dpp<ROR8>(mx));
    mx = swap32_max(swap16_max(mx));
    LDS_WAIT();
    float sv[8], ls = 0.f;
#pragma unroll
    for (int j2 = 0; j2 < 8; ++j2) {
      sv[j2] = pb[(j2 * 2 + (lane & 1)) * 32 + spos];
      ls += __builtin_amdgcn_exp2f((sv[j2] - mx) * C2);
    }
    ls += dpp<XOR1>(ls); ls += dpp<ROR4>(ls); ls += dpp<ROR8>(ls);
    ls = swap16_add(ls, ls);
    ls = swap32_add(ls, ls);
    if (lane == 0 || lane == 2) { LAS float* ml = (LAS float*)(L8 + ML_OFF) + (cw * 2 + hs) * 2; ml[0] = mx; ml[1] = ls; }
    LDS_WAIT();
    lds_inc(C_ML);
    fetch(Ra, TPP + cw, (unsigned)(TPP / NC + 1));
    fetch(Rb, TPP + cw + NC, (unsigned)(TPP / NC + 2));
    wait_ge(C_SCALE, 1u, 1);
    {
      const LAS float* gs = (const LAS float*)(L8 + GS_OFF) + hs * 2;
      const float Mg = gs[0], inv = gs[1];
#pragma unroll
      for (int j2 = 0; j2 < 8; ++j2) {
        const unsigned pbits = bfbits(__builtin_amdgcn_exp2f((sv[j2] - Mg) * C2) * inv);
        pb[(j2 * 2 + (lane & 1)) * 32 + spos] = __uint_as_float(pbits | (pbits << 16));
        if (j2 == 7 && cw >= NC - 2 && (lane & 1)) ((LAS unsigned*)(L8 + Q_OFF))[(cw - (NC - 2)) * 32 + spos] = pbits | (pbits << 16);
      }
      LDS_WAIT();
    }
    pv(Ra, 0);
    pv(Rb, 1);
  }
  u32x4 W[16];
  const auto rs_wo = __builtin_amdgcn_make_buffer_rsrc((void*)(a.wo + (size_t)((2 * kvh + (s >> 1)) * DH + (s & 1) * 128 + 16 * cw) * D), 0, 16 * D * 2, 0x00020000);
  const int wvo = (lane < 56 ? lane : 55) * 16;
  const int fcol = cw * 64 + (lane & 15) * 4 + (lane >> 4);
  unsigned xres = 0;
  if (is_final && cw < 7) xres = __builtin_nontemporal_load((const GAS u16*)a.x + b * D + fcol);
#pragma unroll 1
  for (int j = 2; j < TPP / NC - 1; ++j) {
    if (j >= WJ0 && j < WJ0 + 8) {
#pragma unroll
      for (int i = 0; i < 8; ++i) if (j == i + WJ0) {
        W[2 * i] = __builtin_amdgcn_raw_buffer_load_b128(rs_wo, wvo, (2 * i) * (D * 2), 2);
        W[2 * i + 1] = __builtin_amdgcn_raw_buffer_load_b128(rs_wo, wvo, (2 * i + 1) * (D * 2), 2);
      }
    }
    u32x4 V[8];
    fetch(V, TPP + cw + NC * j, (unsigned)(TPP / NC + j + 1));
    pv(V, j);
  }
  {
#pragma unroll
    for (int i = 0; i < 8; ++i)
#pragma unroll
      for (int k = 0; k < 4; ++k) {
        const unsigned w0 = W[2 * i][k], w1 = W[2 * i + 1][k];
        W[2 * i][k] = __builtin_amdgcn_perm(w1, w0, 0x05040100u);
        W[2 * i + 1][k] = __builtin_amdgcn_perm(w1, w0, 0x07060302u);
      }
    asm volatile("" : "+v"(W[0]), "+v"(W[1]), "+v"(W[2]), "+v"(W[3]), "+v"(W[4]), "+v"(W[5]), "+v"(W[6]), "+v"(W[7]), "+v"(xres));
    asm volatile("" : "+v"(W[8]), "+v"(W[9]), "+v"(W[10]), "+v"(W[11]), "+v"(W[12]), "+v"(W[13]), "+v"(W[14]), "+v"(W[15]));
    wait_ge(C_DONE + NC - 2, (unsigned)(TPP / NC + 3), 1);
    wait_ge(C_DONE + NC - 1, (unsigned)(TPP / NC + 3), 1);
    const LAS unsigned* sp2 = (const LAS unsigned*)(L8 + Q_OFF) + par * 16 + ((lane >> 3) & 1) * 8 + cw;
    const unsigned pd0 = sp2[0], pd1 = sp2[32];
    if (cw < NC - 2) {
      u32x4 V[8];
      fetch(V, NTILES - NC + cw, (unsigned)(NTILES / NC));
      pv(V, TPP / NC - 1);
    }
    wait_tile(NTILES - 2, true);
    pv1(pd0, *(const LAS u32x4*)(L8 + RING_OFF + ((NTILES - 2) & (NS - 1)) * TILE_BYTES + cw * 1024 + lane * 16));
    wait_tile(NTILES - 1, false);
    pv1(pd1, *(const LAS u32x4*)(L8 + RING_OFF + ((NTILES - 1) & (NS - 1)) * TILE_BYTES + cw * 1024 + lane * 16));
    LAS float* rec = pb + c * 8 + par * 4;
    *(LAS f32x4*)rec = f32x4{acc[0], acc[1], acc[2], acc[3]};
    *(LAS f32x4*)(rec + 256) = f32x4{acc[4], acc[5], acc[6], acc[7]};
  }
  BARRIER();
  if (ct < 128) {
    f32x4 o = *(const LAS f32x4*)((const LAS float*)(L8 + PB_OFF) + ct * 4);
#pragma unroll
    for (int w = 1; w < NC; ++w) o += *(const LAS f32x4*)((const LAS float*)(L8 + PB_OFF) + w * 512 + ct * 4);
    __builtin_amdgcn_raw_buffer_store_b128(u32x4{nosent(o[0]), nosent(o[1]), nosent(o[2]), nosent(o[3])}, rs_ws, (int)WS_OB + bid * OB_BYTES + OB_O + ct * 16, 0, 16);
  }
  {
    const int off = (int)WS_OB + (kvh + 8 * b + 64 * (lane >> 4)) * OB_BYTES + OB_O + (s * 128 + cw * 16 + (lane & 15)) * 4;
    unsigned ov = SENT;
    {
      const u64 t0 = rt(); unsigned sp = 0;
      __builtin_amdgcn_s_sleep(10);
      for (;;) {
        if (ov == SENT) ov = __builtin_amdgcn_raw_buffer_load_b32(rs_ws, off, 0, 16);
        if (__all(ov != SENT)) break;
        sp += 15u;
        if (expired(t0, sp)) break;
      }
    }
    float ot = __uint_as_float(ov);
    ot = swap16_add(ot, ot); ot = swap32_add(ot, ot);
    const float ob = rbf(ot);
    const unsigned opair = (__float_as_uint(ob) >> 16) | __float_as_uint(dpp<XOR1>(ob));
    float pa[8];
#pragma unroll
    for (int k = 0; k < 8; ++k) pa[k] = 0.f;
#pragma unroll
    for (int i = 0; i < 8; ++i) {
      const unsigned op = (unsigned)__builtin_amdgcn_readlane((int)opair, 2 * i);
#pragma unroll
      for (int k = 0; k < 4; ++k) { pa[2 * k] = dot2(W[2 * i][k], op, pa[2 * k]); pa[2 * k + 1] = dot2(W[2 * i + 1][k], op, pa[2 * k + 1]); }
    }
    if (lane < 56) {
      LAS float* red = (LAS float*)(L8 + RED_OFF) + cw * D + lane * 8;
      *(LAS f32x4*)red = f32x4{pa[0], pa[1], pa[2], pa[3]};
      *(LAS f32x4*)(red + 4) = f32x4{pa[4], pa[5], pa[6], pa[7]};
    }
  }
  BARRIER();
  if (ct < 112) {
    f32x4 v = *(const LAS f32x4*)((const LAS float*)(L8 + RED_OFF) + ct * 4);
#pragma unroll
    for (int w = 1; w < NC; ++w) v += *(const LAS f32x4*)((const LAS float*)(L8 + RED_OFF) + w * D + ct * 4);
    __builtin_amdgcn_raw_buffer_store_b128(u32x4{nosent(v[0]), nosent(v[1]), nosent(v[2]), nosent(v[3])}, rs_ws, (int)WS_OB + bid * OB_BYTES + OB_P + ct * 16, 0, 16);
  }
  if (!is_final || cw == 7) return;
  {
    const int off = (int)WS_OB + (8 * b + 64 * (lane >> 4)) * OB_BYTES + OB_P + (cw * 64 + (lane & 15) * 4) * 4;
    u32x4 v[8];
#pragma unroll
    for (int i = 0; i < 8; ++i) v[i] = u32x4{SENT, SENT, SENT, SENT};
    {
      const u64 t0 = rt(); unsigned sp = 0;
      for (;;) {
        bool ok = true;
#pragma unroll
        for (int i = 0; i < 8; ++i) if (!full4(v[i])) v[i] = __builtin_amdgcn_raw_buffer_load_b128(rs_ws, off + i * OB_BYTES, 0, 16);
#pragma unroll
        for (int i = 0; i < 8; ++i) ok &= full4(v[i]);
        if (__all(ok)) break;
        sp += 15u;
        if (expired(t0, sp)) break;
      }
    }
    f32x4 sum = __builtin_bit_cast(f32x4, v[0]);
#pragma unroll
    for (int i = 1; i < 8; ++i) sum += __builtin_bit_cast(f32x4, v[i]);
    const float e0 = swap32_add(sum[0], sum[2]), e1 = swap32_add(sum[1], sum[3]);
    const float att = swap16_add(e0, e1);
    const float h = rbf(bf_lo(xres) + rbf(att));
    float t = h;
#pragma unroll
    for (int r = 0; r < 2; ++r) {
      const float cs = first_lane(t), d = t - cs;
      const float s1 = wave_sum(d), s2 = wave_sum(d * d);
      LAS float* ex = (LAS float*)(L8 + EX_OFF) + r * 16;
      if (lane == 0) { ex[cw * 2] = fmaf(s1, 1.f / 64.f, cs); ex[cw * 2 + 1] = fmaxf(fmaf(-s1 * (1.f / 64.f), s1, s2), 0.f); }
      BARRIER();
      const f32x4 x0 = *(const LAS f32x4*)ex, x1 = *(const LAS f32x4*)(ex + 4), x2 = *(const LAS f32x4*)(ex + 8);
      const float m6 = ex[12], q6 = ex[13];
      const float mean = (((x0[0] + x0[2]) + (x1[0] + x1[2])) + ((x2[0] + x2[2]) + m6)) * (1.f / 7.f);
      const float d0 = x0[0] - mean, d1 = x0[2] - mean, d2 = x1[0] - mean, d3 = x1[2] - mean, d4 = x2[0] - mean, d5 = x2[2] - mean, d6 = m6 - mean;
      const float dv = ((d0 * d0 + d1 * d1) + (d2 * d2 + d3 * d3)) + ((d4 * d4 + d5 * d5) + d6 * d6);
      const float m2 = (((x0[1] + x0[3]) + (x1[1] + x1[3])) + ((x2[1] + x2[3]) + q6)) + 64.f * dv;
      t = ln_apply(t, mean, m2 * (1.f / D));
    }
    const unsigned bits = __float_as_uint(t) >> 16;
    const unsigned e = ((bits >> 7) & 0xffu) - (unsigned)E0;
    const bool miss = e >= (unsigned)NE;
    float y = bf1(((const LAS u16*)(L8 + TAB_OFF))[(((bits >> 15) << 7) | (bits & 127u)) * NE + (miss ? 0u : e)]);
    u64 mm = __ballot(miss);
    while (mm) {
      const int src = __builtin_ctzll(mm); mm &= mm - 1;
      const float tv = __builtin_bit_cast(float, __builtin_amdgcn_readlane(__builtin_bit_cast(int, t), src));
      float ya = 0.f;
#pragma unroll 7
      for (int f = lane; f < F; f += 64) ya = fmaf(mixer_unit(tv, bf1(((const GAS u16*)a.w1)[f]), bf1(((const GAS u16*)a.b1)[f])), bf1(((const GAS u16*)a.w2)[f]), ya);
      const float yv = rbf(rbf(wave_sum(ya)) + bf1(*(const GAS u16*)a.b2));
      if (lane == src) y = yv;
    }
    ((GAS u16*)a.out)[b * D + fcol] = (u16)bfbits(h + y);
    if (b == 0 && ct == 0) __hip_atomic_store((GAS u64*)(ws + WS_EPOCH), ((u64)ctl[C_EP] | ((u64)ctl[C_EP + 1] << 32)) + 1ull, RLX_AGENT);
  }
}
}

extern "C" void kernel_launch(void* const* d_in, const int* in_sizes, int n_in, void* d_out, int out_size, void* d_ws, size_t ws_size,
                              hipStream_t stream) {
  Args a;
  a.x = (const u16*)d_in[0]; a.kc = (const u16*)d_in[1]; a.vc = (const u16*)d_in[2]; a.wq = (const u16*)d_in[3]; a.wo = (const u16*)d_in[4];
  a.w1 = (const u16*)d_in[6]; a.b1 = (const u16*)d_in[7]; a.w2 = (const u16*)d_in[8]; a.b2 = (const u16*)d_in[9];
  a.out = (u16*)d_out; a.ws = (unsigned char*)d_ws;
  (void)hipFuncSetAttribute((const void*)k_fused, hipFuncAttributeMaxDynamicSharedMemorySize, LDS_BYTES);
  k_fused<<<NWG, NTHR, LDS_BYTES, stream>>>(a);
}
```
